```python
import torch

def _softmax(x, axis=-1):
    return torch.softmax(x, dim=axis)

def _gelu(x):
    return torch.nn.functional.gelu(x, approximate='tanh')

def _silu(x):
    return torch.nn.functional.silu(x)

def _sigmoid(x):
    return torch.sigmoid(x)

def _layernorm(x, axis=-1, eps=1e-05):
    m = x.mean(dim=axis, keepdim=True)
    v = x.var(dim=axis, keepdim=True, unbiased=False)
    return (x - m) / torch.sqrt(v + eps)

def _scatter_add(tgt, idx, src, axis=0):
    return tgt.clone().index_add_(axis, idx, src)

def _segment_sum(src, seg, n):
    out = torch.zeros((n,) + src.shape[1:], dtype=src.dtype)
    return out.index_add_(0, seg, src)

def _pad(x, pw):
    flat = tuple((v for pair in reversed(pw) for v in pair))
    return torch.nn.functional.pad(x, flat)
kernel_name = 'gather_scatter__molecular_angular_painn__graph'

def setup_inputs():

    _w = lambda a: (lambda t: t.to(torch.bfloat16) if t.is_floating_point() else t)(torch.as_tensor(a))
    N, deg, D = (25000, 16, 128)
    n_rbf = max(16, 4)
    E = N * deg
    cutoff = 5.0
    freqs = (torch.arange(1, n_rbf + 1) * torch.pi / cutoff).to(float)
    return {'sv': _w((lambda *s: torch.randn(*s, dtype=torch.bfloat16) * 0.4)(N, 4, D) * 0.3), 'pos': _w((lambda *s: torch.randn(*s, dtype=torch.bfloat16) * 3.2)(N, 3) * 1.2), 'src': _w((lambda lo, hi, *s: torch.randint(lo, hi, s))(0, N, E).to(torch.int64)), 'dst': _w((lambda lo, hi, *s: torch.randint(lo, hi, s))(0, N, E).to(torch.int64)), 'freqs': _w(freqs), 'w_phi1': _w((lambda *s: torch.randn(*s, dtype=torch.bfloat16) * 3.2)(D, D) / D ** 0.5), 'w_phi2': _w((lambda *s: torch.randn(*s, dtype=torch.bfloat16) * 3.2)(D, 3 * D) / D ** 0.5), 'w_rbf': _w((lambda *s: torch.randn(*s, dtype=torch.bfloat16) * 3.2)(n_rbf, 3 * D) / n_rbf ** 0.5)}

def reference(sv, pos, src, dst, freqs, w_phi1, w_phi2, w_rbf):
    N, _, D = sv.shape
    E = src.shape[0]
    PI, cutoff = (3.141592653589793, 5.0)
    s, v = (sv[:, 0, :].clone(), sv[:, 1:, :].clone())
    r_vec = pos[src] - pos[dst]
    d = torch.sqrt((r_vec * r_vec).sum(-1) + 1e-08)
    r_hat = r_vec / d[:, None]
    rbf = torch.sin(d[:, None] * freqs[None, :]) / d[:, None]
    fcut = 0.5 * (torch.cos(PI * d / cutoff) + 1.0)
    fcut = torch.where(d < cutoff, fcut, 0.0 * d)
    Wf = rbf @ w_rbf * fcut[:, None]
    phi = (_silu(s @ w_phi1) @ w_phi2)[src]
    m = phi * Wf
    m_s, m_vv, m_vr = (m[:, :D].clone(), m[:, D:2 * D].clone(), m[:, 2 * D:].clone())
    ds = _segment_sum(m_s, dst, N)
    dv_e = m_vv[:, None, :] * v[src] + m_vr[:, None, :] * r_hat[:, :, None]
    dv = _segment_sum(dv_e.reshape(E, 3 * D), dst, N).reshape(N, 3, D)
    return sv + torch.concatenate([ds[:, None, :], dv], axis=1)
```

```cpp
#include <hip/hip_runtime.h>
#include <hip/hip_bf16.h>
#include <stdint.h>
#define MIMIC 0
typedef unsigned short u16;
__device__ __forceinline__ float bf2f(u16 x) { return __uint_as_float(((unsigned)x) << 16); }
__device__ __forceinline__ float rbf16(float x) {
  unsigned u = __float_as_uint(x); u += 0x7fffu + ((u >> 16) & 1u); return __uint_as_float(u & 0xffff0000u); }
__device__ __forceinline__ u16 f2bf(float x) { unsigned u = __float_as_uint(x); u += 0x7fffu + ((u >> 16) & 1u); return (u16)(u >> 16); }
__device__ __forceinline__ float RG(float x) { return MIMIC >= 1 ? rbf16(x) : x; }
__device__ __forceinline__ float RM(float x) { return MIMIC >= 2 ? rbf16(x) : x; }

__global__ void k_phi(const u16* sv, const u16* w1, const u16* w2, u16* phi, int N) {
  __shared__ float s[128], h[128];
  int n = blockIdx.x, t = threadIdx.x;
  s[t] = bf2f(sv[(size_t)n * 512 + t]);
  __syncthreads();
  float a = 0.f;
  for (int k = 0; k < 128; ++k) a += s[k] * bf2f(w1[k * 128 + t]);
  a = rbf16(a);
  h[t] = rbf16(a / (1.f + expf(-a)));
  __syncthreads();
  for (int g = 0; g < 3; ++g) {
    float b = 0.f;
    for (int k = 0; k < 128; ++k) b += h[k] * bf2f(w2[k * 384 + g * 128 + t]);
    phi[(size_t)n * 384 + g * 128 + t] = f2bf(b);
  }
}
__global__ void k_link(const int* dst, int* head, int* nxt, int E) {
  int e = blockIdx.x * blockDim.x + threadIdx.x;
  if (e < E) nxt[e] = atomicExch(&head[dst[e]], e);
}
__global__ void k_agg(const u16* sv, const u16* pos, const int* src, const u16* freqs, const u16* wr, const u16* phi,
                      const int* head, const int* nxt, u16* out, int N) {
  int n = blockIdx.x, t = threadIdx.x;
  float acc[4] = {0.f, 0.f, 0.f, 0.f};
  float px = bf2f(pos[n * 3]), py = bf2f(pos[n * 3 + 1]), pz = bf2f(pos[n * 3 + 2]);
  for (int e = head[n]; e >= 0; e = nxt[e]) {
    int s = src[e];
    float rx = RG(bf2f(pos[s * 3]) - px), ry = RG(bf2f(pos[s * 3 + 1]) - py), rz = RG(bf2f(pos[s * 3 + 2]) - pz);
    float ss = RG(RG(rx * rx) + RG(ry * ry) + RG(rz * rz));
    float d = RG(sqrtf(RG(ss + 1e-8f)));
    if (!(d < 5.0f)) continue;
    float hx = RG(rx / d), hy = RG(ry / d), hz = RG(rz / d);
    float fc = RG(0.5f * RG(RG(cosf(RG(RG(3.141592653589793f * d) / 5.0f))) + 1.0f));
    float w[3] = {0.f, 0.f, 0.f};
    for (int k = 0; k < 16; ++k) {
      float r = RG(RG(sinf(RG(d * bf2f(freqs[k])))) / d);
      for (int g = 0; g < 3; ++g) w[g] += r * bf2f(wr[k * 384 + g * 128 + t]);
    }
    float m[3];
    for (int g = 0; g < 3; ++g) m[g] = RM(bf2f(phi[(size_t)s * 384 + g * 128 + t]) * RM(RM(w[g]) * fc));
    acc[0] += m[0];
    acc[1] += RM(RM(m[1] * bf2f(sv[(size_t)s * 512 + 128 + t])) + RM(m[2] * hx));
    acc[2] += RM(RM(m[1] * bf2f(sv[(size_t)s * 512 + 256 + t])) + RM(m[2] * hy));
    acc[3] += RM(RM(m[1] * bf2f(sv[(size_t)s * 512 + 384 + t])) + RM(m[2] * hz));
  }
  for (int c = 0; c < 4; ++c) out[(size_t)n * 512 + c * 128 + t] = f2bf(bf2f(sv[(size_t)n * 512 + c * 128 + t]) + RM(acc[c]));
}
extern "C" void kernel_launch(void* const* d_in, const int* in_sizes, int n_in, void* d_out, int out_size, void* d_ws,
                              size_t ws_size, hipStream_t stream) {
  const u16* sv = (const u16*)d_in[0]; const u16* pos = (const u16*)d_in[1];
  const int* src = (const int*)d_in[2]; const int* dst = (const int*)d_in[3];
  const u16* freqs = (const u16*)d_in[4]; const u16* w1 = (const u16*)d_in[5];
  const u16* w2 = (const u16*)d_in[6]; const u16* wr = (const u16*)d_in[7];
  int N = in_sizes[1] / 3, E = in_sizes[2];
  char* ws = (char*)d_ws;
  int* head = (int*)ws; int* nxt = (int*)(ws + (1 << 20)); u16* phi = (u16*)(ws + (8 << 20));
  hipMemsetAsync(head, 0xff, (size_t)N * 4, stream);
  k_phi<<<N, 128, 0, stream>>>(sv, w1, w2, phi, N);
  k_link<<<(E + 255) / 256, 256, 0, stream>>>(dst, head, nxt, E);
  k_agg<<<N, 128, 0, stream>>>(sv, pos, src, freqs, wr, phi, head, nxt, (u16*)d_out, N);
}
```

```cpp
#include <hip/hip_runtime.h>
#include <stdint.h>

typedef unsigned short u16;
typedef unsigned int u32;
typedef unsigned long long u64;
typedef u32 u32x2 __attribute__((ext_vector_type(2)));
typedef u32 u32x4 __attribute__((ext_vector_type(4)));
typedef float f32x2 __attribute__((ext_vector_type(2)));
typedef float f32x4 __attribute__((ext_vector_type(4)));
typedef short s16x4 __attribute__((ext_vector_type(4)));
typedef u16 u16x2 __attribute__((ext_vector_type(2)));
typedef __bf16 bf16x2 __attribute__((ext_vector_type(2)));
typedef __bf16 bf16x8 __attribute__((ext_vector_type(8)));
typedef __attribute__((address_space(3))) s16x4* lds_s16x4_p;

#pragma clang diagnostic ignored "-Winline-asm"
#define DI __device__ __forceinline__
#define FORCE2 0

constexpr int NBIN = 128;
constexpr int NCELL = 128;
constexpr int CELLW = 16;
constexpr int CELLCAP = 15;
constexpr int NPG_MAX = 112;
constexpr int WUNITS = 14;
constexpr int EPB1 = 3600;
constexpr u32 SRC_MASK = 0x00ffffffu;

constexpr int HB = 4160, TILEB = 33280, FLP = 1040, FLB = 16640, CAPT = 15;
constexpr int L_FL = 99840, L_S = 133120, L_LIST = 137216, L_RBF = 139136, L_DF = 154496, L_RH = 156416, L_MISC = 162240;
#define LOOP_ASM \
  "s_getpc_b64 s[56:57]\n.Lorg%=:\ns_mov_b32 %0, 0\n" \
  "s_mov_b32 %1, 0\ns_mov_b32 s40, 0\ns_mov_b32 s41, 0\n" \
  "s_mov_b32 s43, 0\ns_mov_b32 s46, 0\ns_mov_b32 s47, 0\n" \
  "s_mov_b32 s50, 0\ns_mov_b32 s42, 4\ns_mov_b32 s65, 1\n" \
  "s_add_u32 s61, %7, 0x4000\ns_mov_b64 s[54:55], %3\ns_mov_b32 s66, .Lc0%=-.Lorg%=\n" \
  "s_mov_b32 s67, .Lc1%=-.Lorg%=\ns_mov_b32 s64, .Lcn%=-.Lorg%=\ns_mov_b32 s68, .Ld0%=-.Lorg%=\n" \
  "s_mov_b32 s69, .Ld1%=-.Lorg%=\ns_add_u32 s45, %9, 139136\ns_add_u32 s63, %9, 135168\n" \
  "s_add_u32 s48, %8, 256\nv_mov_b32 v128, v132\ns_mov_b32 s44, 66560\n" \
  "s_waitcnt vmcnt(4) lgkmcnt(0)\ns_barrier\ns_cmp_eq_u32 %2, 0\n" \
  "s_cbranch_scc1 .Lzero%=\nds_read_b128 v[30:33], v128 offset:192\nds_read_b128 v[18:21], v128\n" \
  "ds_read_b128 v[22:25], v128 offset:64\nds_read_b128 v[26:29], v128 offset:128\ns_waitcnt lgkmcnt(0)\n" \
  "v_mfma_f32_16x16x32_bf16 v[10:13], v[18:21], v[170:173], 0\nv_add_u32 v129, s45, v134\nds_read_b128 v[110:113], v129\n" \
  "v_mfma_f32_16x16x32_bf16 v[10:13], v[22:25], v[174:177], v[10:13]\nds_read_b128 v[46:49], v128 offset:16640\nds_read_b128 v[50:53], v128 offset:16704\n" \
  "v_mfma_f32_16x16x32_bf16 v[10:13], v[26:29], v[178:181], v[10:13]\nds_read_b128 v[54:57], v128 offset:16768\nds_read_b128 v[58:61], v128 offset:16832\n" \
  "v_mfma_f32_16x16x32_bf16 v[10:13], v[30:33], v[182:185], v[10:13]\nv_mov_b32 v128, s48\nds_read_b128 v[122:125], v128\n" \
  "v_mfma_f32_16x16x32_bf16 v[6:9], v[18:21], v[154:157], 0\nv_add_u32 v129, s43, v133\ns_add_u32 m0, %10, 0\n" \
  "s_nop 0\nbuffer_load_dwordx4 v134, %4, %7 offen lds\nv_mfma_f32_16x16x32_bf16 v[6:9], v[22:25], v[158:161], v[6:9]\n" \
  "v_mfma_f32_16x16x32_bf16 v[6:9], v[26:29], v[162:165], v[6:9]\ns_add_u32 m0, %10, 16\ns_nop 0\n" \
  "buffer_load_dwordx4 v134, %4, %7 offen offset:1024 lds\nv_mfma_f32_16x16x32_bf16 v[6:9], v[30:33], v[166:169], v[6:9]\nv_mfma_f32_16x16x32_bf16 v[14:17], v[18:21], v[186:189], 0\n" \
  "s_add_u32 m0, %10, 16640\ns_nop 0\nbuffer_load_dwordx4 v134, %4, s61 offen lds\n" \
  "v_mfma_f32_16x16x32_bf16 v[14:17], v[22:25], v[190:193], v[14:17]\nv_mfma_f32_16x16x32_bf16 v[14:17], v[26:29], v[194:197], v[14:17]\ns_add_u32 m0, %10, 16656\n" \
  "s_nop 0\nbuffer_load_dwordx4 v134, %4, s61 offen offset:1024 lds\nv_mfma_f32_16x16x32_bf16 v[14:17], v[30:33], v[198:201], v[14:17]\n" \
  "s_waitcnt lgkmcnt(5)\nv_mfma_f32_16x16x16_bf16 v[18:21], v[110:111], v[202:203], 0\nv_mfma_f32_16x16x16_bf16 v[22:25], v[110:111], v[204:205], 0\n" \
  "v_mfma_f32_16x16x16_bf16 v[26:29], v[110:111], v[206:207], 0\ns_waitcnt lgkmcnt(4)\nv_mfma_f32_16x16x32_bf16 v[38:41], v[46:49], v[170:173], 0\n" \
  "s_add_u32 m0, %6, s44\nds_read_b64_tr_b16 v[64:65], v129\nds_read_b64_tr_b16 v[68:69], v129 offset:256\n" \
  "ds_read_b64_tr_b16 v[72:73], v129 offset:512\ns_waitcnt lgkmcnt(6)\nv_mfma_f32_16x16x32_bf16 v[38:41], v[50:53], v[174:177], v[38:41]\n" \
  "s_add_u32 s42, s42, 4\nds_read_b64_tr_b16 v[76:77], v129 offset:16640\nds_read_b64_tr_b16 v[80:81], v129 offset:16896\n" \
  "ds_read_b64_tr_b16 v[84:85], v129 offset:17152\ns_waitcnt lgkmcnt(8)\nv_mfma_f32_16x16x32_bf16 v[38:41], v[54:57], v[178:181], v[38:41]\n" \
  "s_add_u32 s52, s42, .Lan%=-.Lorg%=\nv_add_u32 v128, s47, v136\nds_read_b64 v[130:131], v128\n" \
  "s_waitcnt lgkmcnt(8)\nv_mfma_f32_16x16x32_bf16 v[38:41], v[58:61], v[182:185], v[38:41]\ns_and_b32 s62, s54, 0xff\n" \
  "v_add_u32 v128, s46, v135\nds_read_b32 v126, v128\nds_read_b32 v127, v128 offset:192\n" \
  "v_mfma_f32_16x16x32_bf16 v[34:37], v[46:49], v[154:157], 0\ns_cmp_gt_u32 s62, s40\ns_cselect_b32 s52, s52, .Lgen1%=-.Lorg%=\n" \
  "s_waitcnt lgkmcnt(8)\nv_lshlrev_b32 v62, 16, v64\nv_lshlrev_b32 v63, 16, v65\n" \
  "s_waitcnt lgkmcnt(5)\nv_lshlrev_b32 v74, 16, v76\nv_mfma_f32_16x16x32_bf16 v[34:37], v[50:53], v[158:161], v[34:37]\n" \
  "s_add_u32 s40, s40, 1\nv_lshlrev_b32 v75, 16, v77\nv_lshlrev_b32 v66, 16, v68\n" \
  "v_mfma_f32_16x16x32_bf16 v[34:37], v[54:57], v[162:165], v[34:37]\ns_cmp_lt_u32 s40, %2\ns_cselect_b32 s52, s52, .Lgen1%=-.Lorg%=\n" \
  "v_lshlrev_b32 v67, 16, v69\ns_waitcnt lgkmcnt(2)\nv_xor_b32 v128, v130, v140\n" \
  "v_add_u32 v129, s50, v137\nv_mfma_f32_16x16x32_bf16 v[34:37], v[58:61], v[166:169], v[34:37]\ns_add_u32 s58, s56, s52\n" \
  "s_addc_u32 s59, s57, 0\nv_pk_sub_u16 v128, 1, v128 op_sel_hi:[0,1] clamp\nv_lshlrev_b32 v78, 16, v80\n" \
  "v_lshlrev_b32 v79, 16, v81\nv_mfma_f32_16x16x32_bf16 v[42:45], v[46:49], v[186:189], 0\ns_lshl_b32 s51, %0, 14\n" \
  "v_lshl_add_u32 v152, v122, v141, v142\nv_lshl_add_u64 v[30:31], v[152:153], 0, v[148:149]\nv_mfma_f32_16x16x32_bf16 v[42:45], v[50:53], v[190:193], v[42:45]\n" \
  "s_add_u32 s51, s51, %7\nglobal_load_lds_dwordx4 v[30:31], off sc1\nv_pk_mul_lo_u16 v128, v128, v131\n" \
  "ds_write_b32 v129, v128\nv_mfma_f32_16x16x32_bf16 v[42:45], v[54:57], v[194:197], v[42:45]\ns_add_u32 s53, s51, 0x10000\n" \
  "v_lshl_add_u32 v152, v123, v141, v143\nv_lshl_add_u64 v[30:31], v[152:153], 0, v[148:149]\nglobal_load_lds_dwordx4 v[30:31], off offset:1024 sc1\n" \
  "v_mfma_f32_16x16x32_bf16 v[42:45], v[58:61], v[198:201], v[42:45]\nv_lshlrev_b32 v70, 16, v72\nv_lshlrev_b32 v71, 16, v73\n" \
  "v_mfma_f32_16x16x16_bf16 v[46:49], v[112:113], v[202:203], 0\ns_mov_b32 s44, s43\ns_add_u32 s43, s43, 33280\n" \
  "s_cmp_eq_u32 s43, 99840\ns_cselect_b32 s43, 0, s43\nv_lshl_add_u32 v152, v124, v141, v144\n" \
  "v_lshl_add_u64 v[30:31], v[152:153], 0, v[148:149]\nglobal_load_lds_dwordx4 v[30:31], off offset:2048 sc1\nv_mfma_f32_16x16x16_bf16 v[50:53], v[112:113], v[204:205], 0\n" \
  "s_add_u32 s45, s45, 1024\ns_add_u32 s46, s46, 384\ns_add_u32 s47, s47, 128\n" \
  "v_lshlrev_b32 v82, 16, v84\nv_lshlrev_b32 v83, 16, v85\nv_lshl_add_u32 v152, v125, v141, v145\n" \
  "v_mfma_f32_16x16x16_bf16 v[54:57], v[112:113], v[206:207], 0\ns_add_u32 s48, s48, 128\ns_xor_b32 s50, s50, 2048\n" \
  "s_sub_u32 s49, s63, s50\nv_lshl_add_u64 v[30:31], v[152:153], 0, v[148:149]\nglobal_load_lds_dwordx4 v[30:31], off offset:3072 sc1\n" \
  "v_add_u32 v128, s43, v132\ns_setpc_b64 s[58:59]\n.Lan%=:\n" \
  "s_waitcnt vmcnt(0)\ns_waitcnt vmcnt(4)\ns_waitcnt vmcnt(8) lgkmcnt(0)\n" \
  "s_barrier\ns_setprio 3\ns_mov_b32 s42, 0\n" \
  "ds_read_b128 v[30:33], v128 offset:192\nv_mul_f32 v6, v6, v18\nv_mul_f32 v7, v7, v19\n" \
  "v_mul_f32 v8, v8, v20\nv_mul_f32 v9, v9, v21\nds_read_b128 v[18:21], v128\n" \
  "v_mul_f32 v10, v10, v22\nv_mul_f32 v11, v11, v23\nv_mul_f32 v12, v12, v24\n" \
  "v_mul_f32 v13, v13, v25\nds_read_b128 v[22:25], v128 offset:64\nv_mul_f32 v14, v14, v26\n" \
  "v_mul_f32 v15, v15, v27\nv_mul_f32 v16, v16, v28\nv_mul_f32 v17, v17, v29\n" \
  "ds_read_b128 v[26:29], v128 offset:128\nv_and_b32 v64, v64, v147\nv_and_b32 v65, v65, v147\n" \
  "v_and_b32 v68, v68, v147\nv_and_b32 v69, v69, v147\nv_and_b32 v72, v72, v147\n" \
  "v_and_b32 v73, v73, v147\nv_and_b32 v76, v76, v147\nv_and_b32 v77, v77, v147\n" \
  "v_and_b32 v80, v80, v147\nv_and_b32 v81, v81, v147\nv_and_b32 v84, v84, v147\n" \
  "v_and_b32 v85, v85, v147\nv_mul_f32 v38, v38, v50\nv_mul_f32 v39, v39, v51\n" \
  "v_mul_f32 v40, v40, v52\nv_mul_f32 v41, v41, v53\nv_mul_f32 v62, v62, v10\n" \
  "v_mul_f32 v64, v64, v11\nv_mul_f32 v63, v63, v12\nv_mul_f32 v65, v65, v13\n" \
  "v_mul_f32 v34, v34, v46\nv_mul_f32 v35, v35, v47\nv_mul_f32 v36, v36, v48\n" \
  "v_mul_f32 v37, v37, v49\nv_mul_f32 v66, v66, v10\nv_mul_f32 v68, v68, v11\n" \
  "v_mul_f32 v67, v67, v12\nv_mul_f32 v69, v69, v13\nv_mul_f32 v42, v42, v54\n" \
  "v_mul_f32 v43, v43, v55\nv_mul_f32 v44, v44, v56\nv_mul_f32 v45, v45, v57\n" \
  "v_mul_f32 v70, v70, v10\nv_mul_f32 v72, v72, v11\nv_mul_f32 v71, v71, v12\n" \
  "v_mul_f32 v73, v73, v13\nv_mul_f32 v74, v74, v38\nv_mul_f32 v76, v76, v39\n" \
  "v_mul_f32 v75, v75, v40\nv_mul_f32 v77, v77, v41\nv_mul_f32 v78, v78, v38\n" \
  "v_mul_f32 v80, v80, v39\nv_mul_f32 v79, v79, v40\nv_mul_f32 v81, v81, v41\n" \
  "v_mul_f32 v82, v82, v38\nv_mul_f32 v84, v84, v39\nv_mul_f32 v83, v83, v40\n" \
  "v_mul_f32 v85, v85, v41\ns_waitcnt lgkmcnt(0)\nv_mfma_f32_16x16x32_bf16 v[10:13], v[18:21], v[170:173], 0\n" \
  "v_cvt_pk_bf16_f32 v86, v6, v7\nv_cvt_pk_bf16_f32 v87, v8, v9\nv_mfma_f32_16x16x32_bf16 v[10:13], v[22:25], v[174:177], v[10:13]\n" \
  "v_fmac_f32_dpp v62, v126, v14 row_newbcast:0 row_mask:0xf bank_mask:0xf\nv_fmac_f32_dpp v64, v126, v15 row_newbcast:1 row_mask:0xf bank_mask:0xf\nv_mfma_f32_16x16x32_bf16 v[10:13], v[26:29], v[178:181], v[10:13]\n" \
  "v_fmac_f32_dpp v63, v126, v16 row_newbcast:2 row_mask:0xf bank_mask:0xf\nv_fmac_f32_dpp v65, v126, v17 row_newbcast:3 row_mask:0xf bank_mask:0xf\nv_mfma_f32_16x16x32_bf16 v[10:13], v[30:33], v[182:185], v[10:13]\n" \
  "s_cmp_lg_u32 s40, s65\ns_cbranch_scc1 .Lr1_%=\ns_add_u32 s60, %7, 0x8000\n" \
  "s_add_u32 s61, %7, 0xc000\ns_mov_b32 s42, 4\nbuffer_load_dwordx4 v[240:243], v134, %4, s60 offen\n" \
  "buffer_load_dwordx4 v[244:247], v134, %4, s60 offen offset:1024\nbuffer_load_dwordx4 v[248:251], v134, %4, s61 offen\nbuffer_load_dwordx4 v[252:255], v134, %4, s61 offen offset:1024\n" \
  ".Lr1_%=:\nv_fmac_f32_dpp v66, v126, v14 row_newbcast:4 row_mask:0xf bank_mask:0xf\nv_fmac_f32_dpp v68, v126, v15 row_newbcast:5 row_mask:0xf bank_mask:0xf\n" \
  "v_mfma_f32_16x16x32_bf16 v[6:9], v[18:21], v[154:157], 0\nv_fmac_f32_dpp v67, v126, v16 row_newbcast:6 row_mask:0xf bank_mask:0xf\nv_fmac_f32_dpp v69, v126, v17 row_newbcast:7 row_mask:0xf bank_mask:0xf\n" \
  "v_mfma_f32_16x16x32_bf16 v[6:9], v[22:25], v[158:161], v[6:9]\ns_mov_b32 s41, %0\nv_fmac_f32_dpp v70, v126, v14 row_newbcast:8 row_mask:0xf bank_mask:0xf\n" \
  "v_fmac_f32_dpp v72, v126, v15 row_newbcast:9 row_mask:0xf bank_mask:0xf\nv_mfma_f32_16x16x32_bf16 v[6:9], v[26:29], v[162:165], v[6:9]\ns_and_b32 s62, s54, 0xff\n" \
  "v_fmac_f32_dpp v71, v126, v16 row_newbcast:10 row_mask:0xf bank_mask:0xf\nv_fmac_f32_dpp v73, v126, v17 row_newbcast:11 row_mask:0xf bank_mask:0xf\nv_mfma_f32_16x16x32_bf16 v[6:9], v[30:33], v[166:169], v[6:9]\n" \
  "s_max_u32 s62, s62, 2\nv_add_u32 v129, s45, v134\nds_read_b128 v[110:113], v129\n" \
  "v_mfma_f32_16x16x32_bf16 v[14:17], v[18:21], v[186:189], 0\ns_add_u32 s52, s40, 2\nds_read_b128 v[46:49], v128 offset:16640\n" \
  "ds_read_b128 v[50:53], v128 offset:16704\nv_mfma_f32_16x16x32_bf16 v[14:17], v[22:25], v[190:193], v[14:17]\ns_cmp_lt_u32 s52, %2\n" \
  "s_cselect_b64 s[60:61], s[66:67], s[68:69]\ns_cselect_b32 s52, s64, .Ldn%=-.Lorg%=\nds_read_b128 v[54:57], v128 offset:16768\n" \
  "ds_read_b128 v[58:61], v128 offset:16832\nv_mfma_f32_16x16x32_bf16 v[14:17], v[26:29], v[194:197], v[14:17]\ns_bitcmp1_b32 %1, 0\n" \
  "s_cselect_b32 s60, s61, s60\nv_add_u32 v128, s49, v134\nds_read_b128 v[102:105], v128\n" \
  "v_mfma_f32_16x16x32_bf16 v[14:17], v[30:33], v[198:201], v[14:17]\ns_cmp_gt_u32 s62, s40\ns_cselect_b32 s52, s52, s60\n" \
  "s_cselect_b32 s62, 0, 8\ns_subb_u32 %1, %1, -1\nds_read_b128 v[106:109], v128 offset:1024\n" \
  "v_cvt_pk_bf16_f32 v88, v34, v35\ns_waitcnt lgkmcnt(6)\nv_mfma_f32_16x16x16_bf16 v[18:21], v[110:111], v[202:203], 0\n" \
  "s_lshr_b64 s[54:55], s[54:55], s62\nv_cvt_pk_bf16_f32 v89, v36, v37\nv_mov_b32 v128, s48\n" \
  "v_mfma_f32_16x16x16_bf16 v[22:25], v[110:111], v[204:205], 0\ns_add_u32 s60, s56, s52\ns_addc_u32 s61, s57, 0\n" \
  "ds_read_b128 v[122:125], v128\nv_add_u32 v129, s43, v133\nv_mfma_f32_16x16x16_bf16 v[26:29], v[110:111], v[206:207], 0\n" \
  "s_setpc_b64 s[60:61]\n.La0%=:\ns_waitcnt vmcnt(0)\n" \
  "s_waitcnt vmcnt(4)\ns_waitcnt vmcnt(8) lgkmcnt(0)\ns_barrier\n" \
  "s_setprio 3\ns_mov_b32 s42, 4\nds_read_b128 v[30:33], v128 offset:192\n" \
  "v_mul_f32 v6, v6, v18\nv_mul_f32 v7, v7, v19\nv_mul_f32 v8, v8, v20\n" \
  "v_mul_f32 v9, v9, v21\nds_read_b128 v[18:21], v128\nv_mul_f32 v10, v10, v22\n" \
  "v_mul_f32 v11, v11, v23\nv_mul_f32 v12, v12, v24\nv_mul_f32 v13, v13, v25\n" \
  "ds_read_b128 v[22:25], v128 offset:64\nv_mul_f32 v14, v14, v26\nv_mul_f32 v15, v15, v27\n" \
  "v_mul_f32 v16, v16, v28\nv_mul_f32 v17, v17, v29\nds_read_b128 v[26:29], v128 offset:128\n" \
  "v_and_b32 v64, v64, v147\nv_and_b32 v65, v65, v147\nv_and_b32 v68, v68, v147\n" \
  "v_and_b32 v69, v69, v147\nds_read_b128 v[114:117], v146\nv_and_b32 v72, v72, v147\n" \
  "v_and_b32 v73, v73, v147\nv_and_b32 v76, v76, v147\nv_and_b32 v77, v77, v147\n" \
  "ds_read_b128 v[118:121], v146 offset:1040\nv_and_b32 v80, v80, v147\nv_and_b32 v81, v81, v147\n" \
  "v_and_b32 v84, v84, v147\nv_and_b32 v85, v85, v147\nv_mul_f32 v38, v38, v50\n" \
  "v_mul_f32 v39, v39, v51\nv_mul_f32 v40, v40, v52\nv_mul_f32 v41, v41, v53\n" \
  "v_mul_f32 v62, v62, v10\nv_mul_f32 v64, v64, v11\nv_mul_f32 v63, v63, v12\n" \
  "v_mul_f32 v65, v65, v13\nv_mul_f32 v34, v34, v46\nv_mul_f32 v35, v35, v47\n" \
  "v_mul_f32 v36, v36, v48\nv_mul_f32 v37, v37, v49\nv_mul_f32 v66, v66, v10\n" \
  "v_mul_f32 v68, v68, v11\nv_mul_f32 v67, v67, v12\nv_mul_f32 v69, v69, v13\n" \
  "v_mul_f32 v42, v42, v54\nv_mul_f32 v43, v43, v55\nv_mul_f32 v44, v44, v56\n" \
  "v_mul_f32 v45, v45, v57\ns_waitcnt lgkmcnt(1)\nbuffer_store_dwordx4 v[114:117], v134, %5, s51 offen nt\n" \
  "v_mul_f32 v70, v70, v10\nv_mul_f32 v72, v72, v11\nv_mul_f32 v71, v71, v12\n" \
  "v_mul_f32 v73, v73, v13\nv_mul_f32 v74, v74, v38\nv_mul_f32 v76, v76, v39\n" \
  "v_mul_f32 v75, v75, v40\nv_mul_f32 v77, v77, v41\ns_waitcnt lgkmcnt(0)\n" \
  "buffer_store_dwordx4 v[118:121], v134, %5, s51 offen offset:1024 nt\nv_mul_f32 v78, v78, v38\nv_mul_f32 v80, v80, v39\n" \
  "v_mul_f32 v79, v79, v40\nv_mul_f32 v81, v81, v41\nv_mul_f32 v82, v82, v38\n" \
  "v_mul_f32 v84, v84, v39\nv_mul_f32 v83, v83, v40\nv_mul_f32 v85, v85, v41\n" \
  "s_waitcnt lgkmcnt(0)\nv_mfma_f32_16x16x32_bf16 v[10:13], v[18:21], v[170:173], 0\nv_cvt_pk_bf16_f32 v86, v6, v7\n" \
  "v_cvt_pk_bf16_f32 v87, v8, v9\nv_mfma_f32_16x16x32_bf16 v[10:13], v[22:25], v[174:177], v[10:13]\nds_write_b128 v146, v[240:243] offset:0\n" \
  "v_fmac_f32_dpp v62, v126, v14 row_newbcast:0 row_mask:0xf bank_mask:0xf\nv_fmac_f32_dpp v64, v126, v15 row_newbcast:1 row_mask:0xf bank_mask:0xf\nv_mfma_f32_16x16x32_bf16 v[10:13], v[26:29], v[178:181], v[10:13]\n" \
  "v_fmac_f32_dpp v63, v126, v16 row_newbcast:2 row_mask:0xf bank_mask:0xf\nv_fmac_f32_dpp v65, v126, v17 row_newbcast:3 row_mask:0xf bank_mask:0xf\nv_mfma_f32_16x16x32_bf16 v[10:13], v[30:33], v[182:185], v[10:13]\n" \
  "buffer_load_dwordx4 v[240:243], v134, %4, s53 offen\nv_fmac_f32_dpp v66, v126, v14 row_newbcast:4 row_mask:0xf bank_mask:0xf\nv_fmac_f32_dpp v68, v126, v15 row_newbcast:5 row_mask:0xf bank_mask:0xf\n" \
  "v_mfma_f32_16x16x32_bf16 v[6:9], v[18:21], v[154:157], 0\nds_write_b128 v146, v[244:247] offset:1040\nv_fmac_f32_dpp v67, v126, v16 row_newbcast:6 row_mask:0xf bank_mask:0xf\n" \
  "v_fmac_f32_dpp v69, v126, v17 row_newbcast:7 row_mask:0xf bank_mask:0xf\nv_mfma_f32_16x16x32_bf16 v[6:9], v[22:25], v[158:161], v[6:9]\ns_mov_b32 s41, %0\n" \
  "s_add_u32 %0, %0, 1\nv_fmac_f32_dpp v70, v126, v14 row_newbcast:8 row_mask:0xf bank_mask:0xf\nv_fmac_f32_dpp v72, v126, v15 row_newbcast:9 row_mask:0xf bank_mask:0xf\n" \
  "v_mfma_f32_16x16x32_bf16 v[6:9], v[26:29], v[162:165], v[6:9]\ns_and_b32 s62, s54, 0xff\nv_fmac_f32_dpp v71, v126, v16 row_newbcast:10 row_mask:0xf bank_mask:0xf\n" \
  "v_fmac_f32_dpp v73, v126, v17 row_newbcast:11 row_mask:0xf bank_mask:0xf\nv_mfma_f32_16x16x32_bf16 v[6:9], v[30:33], v[166:169], v[6:9]\ns_max_u32 s62, s62, 2\n" \
  "v_add_u32 v129, s45, v134\nds_read_b128 v[110:113], v129\nv_mfma_f32_16x16x32_bf16 v[14:17], v[18:21], v[186:189], 0\n" \
  "s_add_u32 s52, s40, 2\nds_read_b128 v[46:49], v128 offset:16640\nds_read_b128 v[50:53], v128 offset:16704\n" \
  "v_mfma_f32_16x16x32_bf16 v[14:17], v[22:25], v[190:193], v[14:17]\ns_cmp_lt_u32 s52, %2\ns_cselect_b64 s[60:61], s[66:67], s[68:69]\n" \
  "s_cselect_b32 s52, s64, .Ldn%=-.Lorg%=\nbuffer_load_dwordx4 v[244:247], v134, %4, s53 offen offset:1024\nds_read_b128 v[54:57], v128 offset:16768\n" \
  "ds_read_b128 v[58:61], v128 offset:16832\nv_mfma_f32_16x16x32_bf16 v[14:17], v[26:29], v[194:197], v[14:17]\ns_bitcmp1_b32 %1, 0\n" \
  "s_cselect_b32 s60, s61, s60\nv_add_u32 v128, s49, v134\nds_read_b128 v[102:105], v128\n" \
  "v_mfma_f32_16x16x32_bf16 v[14:17], v[30:33], v[198:201], v[14:17]\ns_cmp_gt_u32 s62, s40\ns_cselect_b32 s52, s52, s60\n" \
  "s_cselect_b32 s62, 0, 8\ns_subb_u32 %1, %1, -1\nds_read_b128 v[106:109], v128 offset:1024\n" \
  "v_cvt_pk_bf16_f32 v88, v34, v35\ns_waitcnt lgkmcnt(6)\nv_mfma_f32_16x16x16_bf16 v[18:21], v[110:111], v[202:203], 0\n" \
  "s_lshr_b64 s[54:55], s[54:55], s62\nv_cvt_pk_bf16_f32 v89, v36, v37\nv_mov_b32 v128, s48\n" \
  "v_mfma_f32_16x16x16_bf16 v[22:25], v[110:111], v[204:205], 0\ns_add_u32 s60, s56, s52\ns_addc_u32 s61, s57, 0\n" \
  "ds_read_b128 v[122:125], v128\nv_add_u32 v129, s43, v133\nv_mfma_f32_16x16x16_bf16 v[26:29], v[110:111], v[206:207], 0\n" \
  "s_setpc_b64 s[60:61]\n.La1%=:\ns_waitcnt vmcnt(0)\n" \
  "s_waitcnt vmcnt(4)\ns_waitcnt vmcnt(8) lgkmcnt(0)\ns_barrier\n" \
  "s_setprio 3\ns_mov_b32 s42, 4\nds_read_b128 v[30:33], v128 offset:192\n" \
  "v_mul_f32 v6, v6, v18\nv_mul_f32 v7, v7, v19\nv_mul_f32 v8, v8, v20\n" \
  "v_mul_f32 v9, v9, v21\nds_read_b128 v[18:21], v128\nv_mul_f32 v10, v10, v22\n" \
  "v_mul_f32 v11, v11, v23\nv_mul_f32 v12, v12, v24\nv_mul_f32 v13, v13, v25\n" \
  "ds_read_b128 v[22:25], v128 offset:64\nv_mul_f32 v14, v14, v26\nv_mul_f32 v15, v15, v27\n" \
  "v_mul_f32 v16, v16, v28\nv_mul_f32 v17, v17, v29\nds_read_b128 v[26:29], v128 offset:128\n" \
  "v_and_b32 v64, v64, v147\nv_and_b32 v65, v65, v147\nv_and_b32 v68, v68, v147\n" \
  "v_and_b32 v69, v69, v147\nds_read_b128 v[114:117], v146 offset:16640\nv_and_b32 v72, v72, v147\n" \
  "v_and_b32 v73, v73, v147\nv_and_b32 v76, v76, v147\nv_and_b32 v77, v77, v147\n" \
  "ds_read_b128 v[118:121], v146 offset:17680\nv_and_b32 v80, v80, v147\nv_and_b32 v81, v81, v147\n" \
  "v_and_b32 v84, v84, v147\nv_and_b32 v85, v85, v147\nv_mul_f32 v38, v38, v50\n" \
  "v_mul_f32 v39, v39, v51\nv_mul_f32 v40, v40, v52\nv_mul_f32 v41, v41, v53\n" \
  "v_mul_f32 v62, v62, v10\nv_mul_f32 v64, v64, v11\nv_mul_f32 v63, v63, v12\n" \
  "v_mul_f32 v65, v65, v13\nv_mul_f32 v34, v34, v46\nv_mul_f32 v35, v35, v47\n" \
  "v_mul_f32 v36, v36, v48\nv_mul_f32 v37, v37, v49\nv_mul_f32 v66, v66, v10\n" \
  "v_mul_f32 v68, v68, v11\nv_mul_f32 v67, v67, v12\nv_mul_f32 v69, v69, v13\n" \
  "v_mul_f32 v42, v42, v54\nv_mul_f32 v43, v43, v55\nv_mul_f32 v44, v44, v56\n" \
  "v_mul_f32 v45, v45, v57\ns_waitcnt lgkmcnt(1)\nbuffer_store_dwordx4 v[114:117], v134, %5, s51 offen nt\n" \
  "v_mul_f32 v70, v70, v10\nv_mul_f32 v72, v72, v11\nv_mul_f32 v71, v71, v12\n" \
  "v_mul_f32 v73, v73, v13\nv_mul_f32 v74, v74, v38\nv_mul_f32 v76, v76, v39\n" \
  "v_mul_f32 v75, v75, v40\nv_mul_f32 v77, v77, v41\ns_waitcnt lgkmcnt(0)\n" \
  "buffer_store_dwordx4 v[118:121], v134, %5, s51 offen offset:1024 nt\nv_mul_f32 v78, v78, v38\nv_mul_f32 v80, v80, v39\n" \
  "v_mul_f32 v79, v79, v40\nv_mul_f32 v81, v81, v41\nv_mul_f32 v82, v82, v38\n" \
  "v_mul_f32 v84, v84, v39\nv_mul_f32 v83, v83, v40\nv_mul_f32 v85, v85, v41\n" \
  "s_waitcnt lgkmcnt(0)\nv_mfma_f32_16x16x32_bf16 v[10:13], v[18:21], v[170:173], 0\nv_cvt_pk_bf16_f32 v86, v6, v7\n" \
  "v_cvt_pk_bf16_f32 v87, v8, v9\nv_mfma_f32_16x16x32_bf16 v[10:13], v[22:25], v[174:177], v[10:13]\nds_write_b128 v146, v[248:251] offset:16640\n" \
  "v_fmac_f32_dpp v62, v126, v14 row_newbcast:0 row_mask:0xf bank_mask:0xf\nv_fmac_f32_dpp v64, v126, v15 row_newbcast:1 row_mask:0xf bank_mask:0xf\nv_mfma_f32_16x16x32_bf16 v[10:13], v[26:29], v[178:181], v[10:13]\n" \
  "v_fmac_f32_dpp v63, v126, v16 row_newbcast:2 row_mask:0xf bank_mask:0xf\nv_fmac_f32_dpp v65, v126, v17 row_newbcast:3 row_mask:0xf bank_mask:0xf\nv_mfma_f32_16x16x32_bf16 v[10:13], v[30:33], v[182:185], v[10:13]\n" \
  "buffer_load_dwordx4 v[248:251], v134, %4, s53 offen\nv_fmac_f32_dpp v66, v126, v14 row_newbcast:4 row_mask:0xf bank_mask:0xf\nv_fmac_f32_dpp v68, v126, v15 row_newbcast:5 row_mask:0xf bank_mask:0xf\n" \
  "v_mfma_f32_16x16x32_bf16 v[6:9], v[18:21], v[154:157], 0\nds_write_b128 v146, v[252:255] offset:17680\nv_fmac_f32_dpp v67, v126, v16 row_newbcast:6 row_mask:0xf bank_mask:0xf\n" \
  "v_fmac_f32_dpp v69, v126, v17 row_newbcast:7 row_mask:0xf bank_mask:0xf\nv_mfma_f32_16x16x32_bf16 v[6:9], v[22:25], v[158:161], v[6:9]\ns_mov_b32 s41, %0\n" \
  "s_add_u32 %0, %0, 1\nv_fmac_f32_dpp v70, v126, v14 row_newbcast:8 row_mask:0xf bank_mask:0xf\nv_fmac_f32_dpp v72, v126, v15 row_newbcast:9 row_mask:0xf bank_mask:0xf\n" \
  "v_mfma_f32_16x16x32_bf16 v[6:9], v[26:29], v[162:165], v[6:9]\ns_and_b32 s62, s54, 0xff\nv_fmac_f32_dpp v71, v126, v16 row_newbcast:10 row_mask:0xf bank_mask:0xf\n" \
  "v_fmac_f32_dpp v73, v126, v17 row_newbcast:11 row_mask:0xf bank_mask:0xf\nv_mfma_f32_16x16x32_bf16 v[6:9], v[30:33], v[166:169], v[6:9]\ns_max_u32 s62, s62, 2\n" \
  "v_add_u32 v129, s45, v134\nds_read_b128 v[110:113], v129\nv_mfma_f32_16x16x32_bf16 v[14:17], v[18:21], v[186:189], 0\n" \
  "s_add_u32 s52, s40, 2\nds_read_b128 v[46:49], v128 offset:16640\nds_read_b128 v[50:53], v128 offset:16704\n" \
  "v_mfma_f32_16x16x32_bf16 v[14:17], v[22:25], v[190:193], v[14:17]\ns_cmp_lt_u32 s52, %2\ns_cselect_b64 s[60:61], s[66:67], s[68:69]\n" \
  "s_cselect_b32 s52, s64, .Ldn%=-.Lorg%=\nbuffer_load_dwordx4 v[252:255], v134, %4, s53 offen offset:1024\nds_read_b128 v[54:57], v128 offset:16768\n" \
  "ds_read_b128 v[58:61], v128 offset:16832\nv_mfma_f32_16x16x32_bf16 v[14:17], v[26:29], v[194:197], v[14:17]\ns_bitcmp1_b32 %1, 0\n" \
  "s_cselect_b32 s60, s61, s60\nv_add_u32 v128, s49, v134\nds_read_b128 v[102:105], v128\n" \
  "v_mfma_f32_16x16x32_bf16 v[14:17], v[30:33], v[198:201], v[14:17]\ns_cmp_gt_u32 s62, s40\ns_cselect_b32 s52, s52, s60\n" \
  "s_cselect_b32 s62, 0, 8\ns_subb_u32 %1, %1, -1\nds_read_b128 v[106:109], v128 offset:1024\n" \
  "v_cvt_pk_bf16_f32 v88, v34, v35\ns_waitcnt lgkmcnt(6)\nv_mfma_f32_16x16x16_bf16 v[18:21], v[110:111], v[202:203], 0\n" \
  "s_lshr_b64 s[54:55], s[54:55], s62\nv_cvt_pk_bf16_f32 v89, v36, v37\nv_mov_b32 v128, s48\n" \
  "v_mfma_f32_16x16x16_bf16 v[22:25], v[110:111], v[204:205], 0\ns_add_u32 s60, s56, s52\ns_addc_u32 s61, s57, 0\n" \
  "ds_read_b128 v[122:125], v128\nv_add_u32 v129, s43, v133\nv_mfma_f32_16x16x16_bf16 v[26:29], v[110:111], v[206:207], 0\n" \
  "s_setpc_b64 s[60:61]\n.Lcn%=:\ns_waitcnt lgkmcnt(6)\n" \
  "v_mfma_f32_16x16x32_bf16 v[38:41], v[46:49], v[170:173], 0\ns_setprio 2\ns_add_u32 m0, %6, s44\n" \
  "v_cvt_pk_bf16_f32 v90, v62, v64\nv_cvt_pk_bf16_f32 v91, v63, v65\nds_read_b64_tr_b16 v[64:65], v129\n" \
  "s_waitcnt lgkmcnt(6)\nv_mfma_f32_16x16x32_bf16 v[38:41], v[50:53], v[174:177], v[38:41]\ns_add_u32 s42, s42, 4\n" \
  "v_fmac_f32_dpp v74, v127, v42 row_newbcast:0 row_mask:0xf bank_mask:0xf\nv_fmac_f32_dpp v76, v127, v43 row_newbcast:1 row_mask:0xf bank_mask:0xf\nv_fmac_f32_dpp v75, v127, v44 row_newbcast:2 row_mask:0xf bank_mask:0xf\n" \
  "s_waitcnt lgkmcnt(5)\nv_mfma_f32_16x16x32_bf16 v[38:41], v[54:57], v[178:181], v[38:41]\ns_add_u32 s52, s42, .Lan%=-.Lorg%=\n" \
  "v_fmac_f32_dpp v77, v127, v45 row_newbcast:3 row_mask:0xf bank_mask:0xf\nv_cvt_pk_bf16_f32 v92, v74, v76\nv_cvt_pk_bf16_f32 v93, v75, v77\n" \
  "s_waitcnt lgkmcnt(4)\nv_mfma_f32_16x16x32_bf16 v[38:41], v[58:61], v[182:185], v[38:41]\ns_and_b32 s62, s54, 0xff\n" \
  "ds_read_b64_tr_b16 v[76:77], v129 offset:16640\nv_cvt_pk_bf16_f32 v94, v66, v68\nv_cvt_pk_bf16_f32 v95, v67, v69\n" \
  "v_mfma_f32_16x16x32_bf16 v[34:37], v[46:49], v[154:157], 0\ns_cmp_gt_u32 s62, s40\ns_cselect_b32 s52, s52, .Lgen%=-.Lorg%=\n" \
  "ds_read_b64_tr_b16 v[68:69], v129 offset:256\nv_fmac_f32_dpp v78, v127, v42 row_newbcast:4 row_mask:0xf bank_mask:0xf\nv_mfma_f32_16x16x32_bf16 v[34:37], v[50:53], v[158:161], v[34:37]\n" \
  "s_add_u32 s40, s40, 1\nv_fmac_f32_dpp v80, v127, v43 row_newbcast:5 row_mask:0xf bank_mask:0xf\nv_fmac_f32_dpp v79, v127, v44 row_newbcast:6 row_mask:0xf bank_mask:0xf\n" \
  "v_fmac_f32_dpp v81, v127, v45 row_newbcast:7 row_mask:0xf bank_mask:0xf\nv_mfma_f32_16x16x32_bf16 v[34:37], v[54:57], v[162:165], v[34:37]\ns_cmp_lt_u32 s40, %2\n" \
  "s_cselect_b32 s52, s52, .Lgen%=-.Lorg%=\nv_cvt_pk_bf16_f32 v96, v78, v80\nv_cvt_pk_bf16_f32 v97, v79, v81\n" \
  "ds_read_b64_tr_b16 v[80:81], v129 offset:16896\nv_mfma_f32_16x16x32_bf16 v[34:37], v[58:61], v[166:169], v[34:37]\ns_add_u32 s58, s56, s52\n" \
  "s_addc_u32 s59, s57, 0\nv_cvt_pk_bf16_f32 v98, v70, v72\nv_cvt_pk_bf16_f32 v99, v71, v73\n" \
  "ds_read_b64_tr_b16 v[72:73], v129 offset:512\ns_waitcnt lgkmcnt(7)\nv_mfma_f32_16x16x32_bf16 v[208:211], v[86:89], v[102:105], v[208:211]\n" \
  "s_lshl_b32 s51, %0, 14\nv_fmac_f32_dpp v82, v127, v42 row_newbcast:8 row_mask:0xf bank_mask:0xf\nv_fmac_f32_dpp v84, v127, v43 row_newbcast:9 row_mask:0xf bank_mask:0xf\n" \
  "v_fmac_f32_dpp v83, v127, v44 row_newbcast:10 row_mask:0xf bank_mask:0xf\ns_waitcnt lgkmcnt(6)\nv_mfma_f32_16x16x32_bf16 v[224:227], v[86:89], v[106:109], v[224:227]\n" \
  "s_add_u32 s51, s51, %7\ns_setprio 1\nv_fmac_f32_dpp v85, v127, v45 row_newbcast:11 row_mask:0xf bank_mask:0xf\n" \
  "v_cvt_pk_bf16_f32 v100, v82, v84\nv_mfma_f32_16x16x32_bf16 v[212:215], v[90:93], v[102:105], v[212:215]\ns_add_u32 s53, s51, 0x10000\n" \
  "v_cvt_pk_bf16_f32 v101, v83, v85\nds_read_b64_tr_b16 v[84:85], v129 offset:17152\nv_add_u32 v128, s47, v136\n" \
  "v_mfma_f32_16x16x32_bf16 v[228:231], v[90:93], v[106:109], v[228:231]\nds_read_b64 v[130:131], v128\nv_add_u32 v128, s46, v135\n" \
  "ds_read_b32 v126, v128\nv_mfma_f32_16x16x32_bf16 v[216:219], v[94:97], v[102:105], v[216:219]\nds_read_b32 v127, v128 offset:192\n" \
  "s_waitcnt lgkmcnt(8)\nv_lshlrev_b32 v62, 16, v64\nv_lshlrev_b32 v63, 16, v65\n" \
  "v_mfma_f32_16x16x32_bf16 v[232:235], v[94:97], v[106:109], v[232:235]\ns_waitcnt lgkmcnt(7)\nv_lshlrev_b32 v74, 16, v76\n" \
  "v_lshlrev_b32 v75, 16, v77\nv_mfma_f32_16x16x32_bf16 v[220:223], v[98:101], v[102:105], v[220:223]\ns_waitcnt lgkmcnt(6)\n" \
  "v_lshlrev_b32 v66, 16, v68\nv_lshlrev_b32 v67, 16, v69\nv_lshl_add_u32 v152, v122, v141, v142\n" \
  "v_mfma_f32_16x16x32_bf16 v[236:239], v[98:101], v[106:109], v[236:239]\nv_lshl_add_u64 v[30:31], v[152:153], 0, v[148:149]\nglobal_load_lds_dwordx4 v[30:31], off sc1\n" \
  "s_waitcnt lgkmcnt(2)\nv_xor_b32 v128, v130, v140\nv_mfma_f32_16x16x32_bf16 v[42:45], v[46:49], v[186:189], 0\n" \
  "v_add_u32 v129, s50, v137\nv_pk_sub_u16 v128, 1, v128 op_sel_hi:[0,1] clamp\nv_lshl_add_u32 v152, v123, v141, v143\n" \
  "v_mfma_f32_16x16x32_bf16 v[42:45], v[50:53], v[190:193], v[42:45]\ns_setprio 0\nv_lshl_add_u64 v[30:31], v[152:153], 0, v[148:149]\n" \
  "global_load_lds_dwordx4 v[30:31], off offset:1024 sc1\nv_lshlrev_b32 v78, 16, v80\nv_mfma_f32_16x16x32_bf16 v[42:45], v[54:57], v[194:197], v[42:45]\n" \
  "v_lshlrev_b32 v79, 16, v81\nv_pk_mul_lo_u16 v128, v128, v131\nv_mfma_f32_16x16x32_bf16 v[42:45], v[58:61], v[198:201], v[42:45]\n" \
  "ds_write_b32 v129, v128\nv_lshl_add_u32 v152, v124, v141, v144\nv_lshl_add_u64 v[30:31], v[152:153], 0, v[148:149]\n" \
  "v_mfma_f32_16x16x16_bf16 v[46:49], v[112:113], v[202:203], 0\ns_mov_b32 s44, s43\ns_add_u32 s43, s43, 33280\n" \
  "s_cmp_eq_u32 s43, 99840\ns_cselect_b32 s43, 0, s43\nglobal_load_lds_dwordx4 v[30:31], off offset:2048 sc1\n" \
  "v_lshlrev_b32 v70, 16, v72\nv_lshlrev_b32 v71, 16, v73\nv_mfma_f32_16x16x16_bf16 v[50:53], v[112:113], v[204:205], 0\n" \
  "s_add_u32 s45, s45, 1024\ns_add_u32 s46, s46, 384\ns_add_u32 s47, s47, 128\n" \
  "v_lshlrev_b32 v82, 16, v84\nv_lshlrev_b32 v83, 16, v85\nv_lshl_add_u32 v152, v125, v141, v145\n" \
  "v_mfma_f32_16x16x16_bf16 v[54:57], v[112:113], v[206:207], 0\ns_add_u32 s48, s48, 128\ns_xor_b32 s50, s50, 2048\n" \
  "s_sub_u32 s49, s63, s50\nv_lshl_add_u64 v[30:31], v[152:153], 0, v[148:149]\nglobal_load_lds_dwordx4 v[30:31], off offset:3072 sc1\n" \
  "v_add_u32 v128, s43, v132\ns_setpc_b64 s[58:59]\n.Lc0%=:\n" \
  "s_waitcnt lgkmcnt(6)\nv_mfma_f32_16x16x32_bf16 v[38:41], v[46:49], v[170:173], 0\ns_setprio 2\n" \
  "s_add_u32 m0, %6, s44\nds_read_b64_tr_b16 v[114:115], v139\nds_read_b64_tr_b16 v[116:117], v139 offset:256\n" \
  "ds_read_b64_tr_b16 v[118:119], v139 offset:512\nds_read_b64_tr_b16 v[120:121], v139 offset:768\ns_waitcnt lgkmcnt(9)\n" \
  "v_mfma_f32_16x16x32_bf16 v[38:41], v[50:53], v[174:177], v[38:41]\ns_add_u32 s42, s42, 4\nv_cvt_pk_bf16_f32 v90, v62, v64\n" \
  "v_cvt_pk_bf16_f32 v91, v63, v65\nds_read_b64_tr_b16 v[64:65], v129\ns_waitcnt lgkmcnt(9)\n" \
  "v_mfma_f32_16x16x32_bf16 v[38:41], v[54:57], v[178:181], v[38:41]\ns_add_u32 s52, s42, .La0%=-.Lorg%=\nv_fmac_f32_dpp v74, v127, v42 row_newbcast:0 row_mask:0xf bank_mask:0xf\n" \
  "v_fmac_f32_dpp v76, v127, v43 row_newbcast:1 row_mask:0xf bank_mask:0xf\nv_fmac_f32_dpp v75, v127, v44 row_newbcast:2 row_mask:0xf bank_mask:0xf\ns_waitcnt lgkmcnt(8)\n" \
  "v_mfma_f32_16x16x32_bf16 v[38:41], v[58:61], v[182:185], v[38:41]\ns_and_b32 s62, s54, 0xff\nv_fmac_f32_dpp v77, v127, v45 row_newbcast:3 row_mask:0xf bank_mask:0xf\n" \
  "v_cvt_pk_bf16_f32 v92, v74, v76\nv_cvt_pk_bf16_f32 v93, v75, v77\nds_read_b64_tr_b16 v[76:77], v129 offset:16640\n" \
  "v_mfma_f32_16x16x32_bf16 v[34:37], v[46:49], v[154:157], 0\ns_cmp_gt_u32 s62, s40\ns_cselect_b32 s52, s52, .Lgen%=-.Lorg%=\n" \
  "v_cvt_pk_bf16_f32 v94, v66, v68\nv_cvt_pk_bf16_f32 v95, v67, v69\nds_read_b64_tr_b16 v[68:69], v129 offset:256\n" \
  "v_mfma_f32_16x16x32_bf16 v[34:37], v[50:53], v[158:161], v[34:37]\ns_add_u32 s40, s40, 1\nv_fmac_f32_dpp v78, v127, v42 row_newbcast:4 row_mask:0xf bank_mask:0xf\n" \
  "v_fmac_f32_dpp v80, v127, v43 row_newbcast:5 row_mask:0xf bank_mask:0xf\nv_fmac_f32_dpp v79, v127, v44 row_newbcast:6 row_mask:0xf bank_mask:0xf\nv_mfma_f32_16x16x32_bf16 v[34:37], v[54:57], v[162:165], v[34:37]\n" \
  "s_cmp_lt_u32 s40, %2\ns_cselect_b32 s52, s52, .Lgen%=-.Lorg%=\nv_fmac_f32_dpp v81, v127, v45 row_newbcast:7 row_mask:0xf bank_mask:0xf\n" \
  "v_cvt_pk_bf16_f32 v96, v78, v80\nv_cvt_pk_bf16_f32 v97, v79, v81\nv_mfma_f32_16x16x32_bf16 v[34:37], v[58:61], v[166:169], v[34:37]\n" \
  "s_add_u32 s58, s56, s52\ns_addc_u32 s59, s57, 0\nds_read_b64_tr_b16 v[80:81], v129 offset:16896\n" \
  "v_cvt_pk_bf16_f32 v98, v70, v72\nv_cvt_pk_bf16_f32 v99, v71, v73\nds_read_b64_tr_b16 v[72:73], v129 offset:512\n" \
  "s_waitcnt lgkmcnt(8)\nv_mfma_f32_16x16x16_bf16 v[208:211], v[114:115], v[150:151], v[208:211]\ns_lshl_b32 s51, %0, 14\n" \
  "v_fmac_f32_dpp v82, v127, v42 row_newbcast:8 row_mask:0xf bank_mask:0xf\nv_fmac_f32_dpp v84, v127, v43 row_newbcast:9 row_mask:0xf bank_mask:0xf\nv_fmac_f32_dpp v83, v127, v44 row_newbcast:10 row_mask:0xf bank_mask:0xf\n" \
  "s_waitcnt lgkmcnt(7)\nv_mfma_f32_16x16x16_bf16 v[212:215], v[116:117], v[150:151], v[212:215]\ns_add_u32 s51, s51, %7\n" \
  "v_fmac_f32_dpp v85, v127, v45 row_newbcast:11 row_mask:0xf bank_mask:0xf\nv_cvt_pk_bf16_f32 v100, v82, v84\nv_cvt_pk_bf16_f32 v101, v83, v85\n" \
  "s_waitcnt lgkmcnt(6)\nv_mfma_f32_16x16x16_bf16 v[216:219], v[118:119], v[150:151], v[216:219]\ns_add_u32 s53, s51, 0x10000\n" \
  "ds_read_b64_tr_b16 v[84:85], v129 offset:17152\nv_add_u32 v128, s47, v136\nds_read_b64 v[130:131], v128\n" \
  "s_waitcnt lgkmcnt(7)\nv_mfma_f32_16x16x16_bf16 v[220:223], v[120:121], v[150:151], v[220:223]\nv_add_u32 v128, s46, v135\n" \
  "ds_read_b32 v126, v128\nds_read_b32 v127, v128 offset:192\ns_waitcnt lgkmcnt(8)\n" \
  "v_lshlrev_b32 v62, 16, v64\nv_mfma_f32_16x16x32_bf16 v[208:211], v[86:89], v[102:105], v[208:211]\nv_lshlrev_b32 v63, 16, v65\n" \
  "s_waitcnt lgkmcnt(7)\nv_lshlrev_b32 v74, 16, v76\nv_lshlrev_b32 v75, 16, v77\n" \
  "v_mfma_f32_16x16x32_bf16 v[224:227], v[86:89], v[106:109], v[224:227]\ns_setprio 1\ns_waitcnt lgkmcnt(6)\n" \
  "v_lshlrev_b32 v66, 16, v68\nv_lshlrev_b32 v67, 16, v69\ns_waitcnt lgkmcnt(2)\n" \
  "v_xor_b32 v128, v130, v140\nv_mfma_f32_16x16x32_bf16 v[212:215], v[90:93], v[102:105], v[212:215]\nv_add_u32 v129, s50, v137\n" \
  "v_pk_sub_u16 v128, 1, v128 op_sel_hi:[0,1] clamp\nv_lshlrev_b32 v78, 16, v80\nv_mfma_f32_16x16x32_bf16 v[228:231], v[90:93], v[106:109], v[228:231]\n" \
  "v_lshlrev_b32 v79, 16, v81\nv_pk_mul_lo_u16 v128, v128, v131\nds_write_b32 v129, v128\n" \
  "v_lshlrev_b32 v70, 16, v72\nv_mfma_f32_16x16x32_bf16 v[216:219], v[94:97], v[102:105], v[216:219]\nv_lshlrev_b32 v71, 16, v73\n" \
  "v_lshlrev_b32 v82, 16, v84\nv_lshlrev_b32 v83, 16, v85\nv_mfma_f32_16x16x32_bf16 v[232:235], v[94:97], v[106:109], v[232:235]\n" \
  "v_mfma_f32_16x16x32_bf16 v[220:223], v[98:101], v[102:105], v[220:223]\nv_mfma_f32_16x16x32_bf16 v[236:239], v[98:101], v[106:109], v[236:239]\nv_cvt_pk_bf16_f32 v102, v208, v209\n" \
  "v_cvt_pk_bf16_f32 v103, v210, v211\nds_write_b64 v138, v[102:103] offset:0\nv_mov_b64 v[208:209], 0\n" \
  "v_mfma_f32_16x16x32_bf16 v[42:45], v[46:49], v[186:189], 0\nv_lshl_add_u32 v152, v122, v141, v142\nv_lshl_add_u64 v[30:31], v[152:153], 0, v[148:149]\n" \
  "global_load_lds_dwordx4 v[30:31], off sc1\nv_mov_b64 v[210:211], 0\nv_mfma_f32_16x16x32_bf16 v[42:45], v[50:53], v[190:193], v[42:45]\n" \
  "s_setprio 0\nv_cvt_pk_bf16_f32 v104, v212, v213\nv_cvt_pk_bf16_f32 v105, v214, v215\n" \
  "ds_write_b64 v138, v[104:105] offset:256\nv_mov_b64 v[212:213], 0\nv_mfma_f32_16x16x32_bf16 v[42:45], v[54:57], v[194:197], v[42:45]\n" \
  "v_lshl_add_u32 v152, v123, v141, v143\nv_lshl_add_u64 v[30:31], v[152:153], 0, v[148:149]\nglobal_load_lds_dwordx4 v[30:31], off offset:1024 sc1\n" \
  "v_mov_b64 v[214:215], 0\nv_mfma_f32_16x16x32_bf16 v[42:45], v[58:61], v[198:201], v[42:45]\nv_cvt_pk_bf16_f32 v106, v216, v217\n" \
  "v_cvt_pk_bf16_f32 v107, v218, v219\nds_write_b64 v138, v[106:107] offset:512\nv_mov_b64 v[216:217], 0\n" \
  "v_mfma_f32_16x16x16_bf16 v[46:49], v[112:113], v[202:203], 0\ns_mov_b32 s44, s43\ns_add_u32 s43, s43, 33280\n" \
  "s_cmp_eq_u32 s43, 99840\ns_cselect_b32 s43, 0, s43\nv_lshl_add_u32 v152, v124, v141, v144\n" \
  "v_lshl_add_u64 v[30:31], v[152:153], 0, v[148:149]\nglobal_load_lds_dwordx4 v[30:31], off offset:2048 sc1\nv_mov_b64 v[218:219], 0\n" \
  "v_mfma_f32_16x16x16_bf16 v[50:53], v[112:113], v[204:205], 0\ns_add_u32 s45, s45, 1024\ns_add_u32 s46, s46, 384\n" \
  "s_add_u32 s47, s47, 128\nv_cvt_pk_bf16_f32 v108, v220, v221\nv_cvt_pk_bf16_f32 v109, v222, v223\n" \
  "ds_write_b64 v138, v[108:109] offset:768\nv_mov_b64 v[220:221], 0\nv_mfma_f32_16x16x16_bf16 v[54:57], v[112:113], v[206:207], 0\n" \
  "s_add_u32 s48, s48, 128\ns_xor_b32 s50, s50, 2048\ns_sub_u32 s49, s63, s50\n" \
  "v_mov_b64 v[222:223], 0\nv_lshl_add_u32 v152, v125, v141, v145\nv_lshl_add_u64 v[30:31], v[152:153], 0, v[148:149]\n" \
  "global_load_lds_dwordx4 v[30:31], off offset:3072 sc1\nv_add_u32 v128, s43, v132\ns_setpc_b64 s[58:59]\n" \
  ".Lc1%=:\ns_waitcnt lgkmcnt(6)\nv_mfma_f32_16x16x32_bf16 v[38:41], v[46:49], v[170:173], 0\n" \
  "s_setprio 2\ns_add_u32 m0, %6, s44\nds_read_b64_tr_b16 v[114:115], v139 offset:16640\n" \
  "ds_read_b64_tr_b16 v[116:117], v139 offset:16896\nds_read_b64_tr_b16 v[118:119], v139 offset:17152\nds_read_b64_tr_b16 v[120:121], v139 offset:17408\n" \
  "s_waitcnt lgkmcnt(9)\nv_mfma_f32_16x16x32_bf16 v[38:41], v[50:53], v[174:177], v[38:41]\ns_add_u32 s42, s42, 4\n" \
  "v_cvt_pk_bf16_f32 v90, v62, v64\nv_cvt_pk_bf16_f32 v91, v63, v65\nds_read_b64_tr_b16 v[64:65], v129\n" \
  "s_waitcnt lgkmcnt(9)\nv_mfma_f32_16x16x32_bf16 v[38:41], v[54:57], v[178:181], v[38:41]\ns_add_u32 s52, s42, .La1%=-.Lorg%=\n" \
  "v_fmac_f32_dpp v74, v127, v42 row_newbcast:0 row_mask:0xf bank_mask:0xf\nv_fmac_f32_dpp v76, v127, v43 row_newbcast:1 row_mask:0xf bank_mask:0xf\nv_fmac_f32_dpp v75, v127, v44 row_newbcast:2 row_mask:0xf bank_mask:0xf\n" \
  "s_waitcnt lgkmcnt(8)\nv_mfma_f32_16x16x32_bf16 v[38:41], v[58:61], v[182:185], v[38:41]\ns_and_b32 s62, s54, 0xff\n" \
  "v_fmac_f32_dpp v77, v127, v45 row_newbcast:3 row_mask:0xf bank_mask:0xf\nv_cvt_pk_bf16_f32 v92, v74, v76\nv_cvt_pk_bf16_f32 v93, v75, v77\n" \
  "ds_read_b64_tr_b16 v[76:77], v129 offset:16640\nv_mfma_f32_16x16x32_bf16 v[34:37], v[46:49], v[154:157], 0\ns_cmp_gt_u32 s62, s40\n" \
  "s_cselect_b32 s52, s52, .Lgen%=-.Lorg%=\nv_cvt_pk_bf16_f32 v94, v66, v68\nv_cvt_pk_bf16_f32 v95, v67, v69\n" \
  "ds_read_b64_tr_b16 v[68:69], v129 offset:256\nv_mfma_f32_16x16x32_bf16 v[34:37], v[50:53], v[158:161], v[34:37]\ns_add_u32 s40, s40, 1\n" \
  "v_fmac_f32_dpp v78, v127, v42 row_newbcast:4 row_mask:0xf bank_mask:0xf\nv_fmac_f32_dpp v80, v127, v43 row_newbcast:5 row_mask:0xf bank_mask:0xf\nv_fmac_f32_dpp v79, v127, v44 row_newbcast:6 row_mask:0xf bank_mask:0xf\n" \
  "v_mfma_f32_16x16x32_bf16 v[34:37], v[54:57], v[162:165], v[34:37]\ns_cmp_lt_u32 s40, %2\ns_cselect_b32 s52, s52, .Lgen%=-.Lorg%=\n" \
  "v_fmac_f32_dpp v81, v127, v45 row_newbcast:7 row_mask:0xf bank_mask:0xf\nv_cvt_pk_bf16_f32 v96, v78, v80\nv_cvt_pk_bf16_f32 v97, v79, v81\n" \
  "v_mfma_f32_16x16x32_bf16 v[34:37], v[58:61], v[166:169], v[34:37]\ns_add_u32 s58, s56, s52\ns_addc_u32 s59, s57, 0\n" \
  "ds_read_b64_tr_b16 v[80:81], v129 offset:16896\nv_cvt_pk_bf16_f32 v98, v70, v72\nv_cvt_pk_bf16_f32 v99, v71, v73\n" \
  "ds_read_b64_tr_b16 v[72:73], v129 offset:512\ns_waitcnt lgkmcnt(8)\nv_mfma_f32_16x16x16_bf16 v[224:227], v[114:115], v[150:151], v[224:227]\n" \
  "s_lshl_b32 s51, %0, 14\nv_fmac_f32_dpp v82, v127, v42 row_newbcast:8 row_mask:0xf bank_mask:0xf\nv_fmac_f32_dpp v84, v127, v43 row_newbcast:9 row_mask:0xf bank_mask:0xf\n" \
  "v_fmac_f32_dpp v83, v127, v44 row_newbcast:10 row_mask:0xf bank_mask:0xf\ns_waitcnt lgkmcnt(7)\nv_mfma_f32_16x16x16_bf16 v[228:231], v[116:117], v[150:151], v[228:231]\n" \
  "s_add_u32 s51, s51, %7\nv_fmac_f32_dpp v85, v127, v45 row_newbcast:11 row_mask:0xf bank_mask:0xf\nv_cvt_pk_bf16_f32 v100, v82, v84\n" \
  "v_cvt_pk_bf16_f32 v101, v83, v85\ns_waitcnt lgkmcnt(6)\nv_mfma_f32_16x16x16_bf16 v[232:235], v[118:119], v[150:151], v[232:235]\n" \
  "s_add_u32 s53, s51, 0x10000\nds_read_b64_tr_b16 v[84:85], v129 offset:17152\nv_add_u32 v128, s47, v136\n" \
  "ds_read_b64 v[130:131], v128\ns_waitcnt lgkmcnt(7)\nv_mfma_f32_16x16x16_bf16 v[236:239], v[120:121], v[150:151], v[236:239]\n" \
  "v_add_u32 v128, s46, v135\nds_read_b32 v126, v128\nds_read_b32 v127, v128 offset:192\n" \
  "s_waitcnt lgkmcnt(8)\nv_lshlrev_b32 v62, 16, v64\nv_mfma_f32_16x16x32_bf16 v[208:211], v[86:89], v[102:105], v[208:211]\n" \
  "v_lshlrev_b32 v63, 16, v65\ns_waitcnt lgkmcnt(7)\nv_lshlrev_b32 v74, 16, v76\n" \
  "v_lshlrev_b32 v75, 16, v77\nv_mfma_f32_16x16x32_bf16 v[224:227], v[86:89], v[106:109], v[224:227]\ns_setprio 1\n" \
  "s_waitcnt lgkmcnt(6)\nv_lshlrev_b32 v66, 16, v68\nv_lshlrev_b32 v67, 16, v69\n" \
  "s_waitcnt lgkmcnt(2)\nv_xor_b32 v128, v130, v140\nv_mfma_f32_16x16x32_bf16 v[212:215], v[90:93], v[102:105], v[212:215]\n" \
  "v_add_u32 v129, s50, v137\nv_pk_sub_u16 v128, 1, v128 op_sel_hi:[0,1] clamp\nv_lshlrev_b32 v78, 16, v80\n" \
  "v_mfma_f32_16x16x32_bf16 v[228:231], v[90:93], v[106:109], v[228:231]\nv_lshlrev_b32 v79, 16, v81\nv_pk_mul_lo_u16 v128, v128, v131\n" \
  "ds_write_b32 v129, v128\nv_lshlrev_b32 v70, 16, v72\nv_mfma_f32_16x16x32_bf16 v[216:219], v[94:97], v[102:105], v[216:219]\n" \
  "v_lshlrev_b32 v71, 16, v73\nv_lshlrev_b32 v82, 16, v84\nv_lshlrev_b32 v83, 16, v85\n" \
  "v_mfma_f32_16x16x32_bf16 v[232:235], v[94:97], v[106:109], v[232:235]\nv_mfma_f32_16x16x32_bf16 v[220:223], v[98:101], v[102:105], v[220:223]\nv_mfma_f32_16x16x32_bf16 v[236:239], v[98:101], v[106:109], v[236:239]\n" \
  "v_cvt_pk_bf16_f32 v102, v224, v225\nv_cvt_pk_bf16_f32 v103, v226, v227\nds_write_b64 v138, v[102:103] offset:16640\n" \
  "v_mov_b64 v[224:225], 0\nv_mfma_f32_16x16x32_bf16 v[42:45], v[46:49], v[186:189], 0\nv_lshl_add_u32 v152, v122, v141, v142\n" \
  "v_lshl_add_u64 v[30:31], v[152:153], 0, v[148:149]\nglobal_load_lds_dwordx4 v[30:31], off sc1\nv_mov_b64 v[226:227], 0\n" \
  "v_mfma_f32_16x16x32_bf16 v[42:45], v[50:53], v[190:193], v[42:45]\ns_setprio 0\nv_cvt_pk_bf16_f32 v104, v228, v229\n" \
  "v_cvt_pk_bf16_f32 v105, v230, v231\nds_write_b64 v138, v[104:105] offset:16896\nv_mov_b64 v[228:229], 0\n" \
  "v_mfma_f32_16x16x32_bf16 v[42:45], v[54:57], v[194:197], v[42:45]\nv_lshl_add_u32 v152, v123, v141, v143\nv_lshl_add_u64 v[30:31], v[152:153], 0, v[148:149]\n" \
  "global_load_lds_dwordx4 v[30:31], off offset:1024 sc1\nv_mov_b64 v[230:231], 0\nv_mfma_f32_16x16x32_bf16 v[42:45], v[58:61], v[198:201], v[42:45]\n" \
  "v_cvt_pk_bf16_f32 v106, v232, v233\nv_cvt_pk_bf16_f32 v107, v234, v235\nds_write_b64 v138, v[106:107] offset:17152\n" \
  "v_mov_b64 v[232:233], 0\nv_mfma_f32_16x16x16_bf16 v[46:49], v[112:113], v[202:203], 0\ns_mov_b32 s44, s43\n" \
  "s_add_u32 s43, s43, 33280\ns_cmp_eq_u32 s43, 99840\ns_cselect_b32 s43, 0, s43\n" \
  "v_lshl_add_u32 v152, v124, v141, v144\nv_lshl_add_u64 v[30:31], v[152:153], 0, v[148:149]\nglobal_load_lds_dwordx4 v[30:31], off offset:2048 sc1\n" \
  "v_mov_b64 v[234:235], 0\nv_mfma_f32_16x16x16_bf16 v[50:53], v[112:113], v[204:205], 0\ns_add_u32 s45, s45, 1024\n" \
  "s_add_u32 s46, s46, 384\ns_add_u32 s47, s47, 128\nv_cvt_pk_bf16_f32 v108, v236, v237\n" \
  "v_cvt_pk_bf16_f32 v109, v238, v239\nds_write_b64 v138, v[108:109] offset:17408\nv_mov_b64 v[236:237], 0\n" \
  "v_mfma_f32_16x16x16_bf16 v[54:57], v[112:113], v[206:207], 0\ns_add_u32 s48, s48, 128\ns_xor_b32 s50, s50, 2048\n" \
  "s_sub_u32 s49, s63, s50\nv_mov_b64 v[238:239], 0\nv_lshl_add_u32 v152, v125, v141, v145\n" \
  "v_lshl_add_u64 v[30:31], v[152:153], 0, v[148:149]\nglobal_load_lds_dwordx4 v[30:31], off offset:3072 sc1\nv_add_u32 v128, s43, v132\n" \
  "s_setpc_b64 s[58:59]\n.Ldn%=:\ns_waitcnt lgkmcnt(6)\n" \
  "v_mfma_f32_16x16x32_bf16 v[38:41], v[46:49], v[170:173], 0\ns_setprio 2\nv_cvt_pk_bf16_f32 v90, v62, v64\n" \
  "v_cvt_pk_bf16_f32 v91, v63, v65\nds_read_b64_tr_b16 v[64:65], v129\ns_waitcnt lgkmcnt(6)\n" \
  "v_mfma_f32_16x16x32_bf16 v[38:41], v[50:53], v[174:177], v[38:41]\ns_add_u32 s52, s42, .Lan%=-.Lorg%=\nv_fmac_f32_dpp v74, v127, v42 row_newbcast:0 row_mask:0xf bank_mask:0xf\n" \
  "v_fmac_f32_dpp v76, v127, v43 row_newbcast:1 row_mask:0xf bank_mask:0xf\ns_waitcnt lgkmcnt(5)\nv_mfma_f32_16x16x32_bf16 v[38:41], v[54:57], v[178:181], v[38:41]\n" \
  "s_and_b32 s62, s54, 0xff\nv_fmac_f32_dpp v75, v127, v44 row_newbcast:2 row_mask:0xf bank_mask:0xf\nv_fmac_f32_dpp v77, v127, v45 row_newbcast:3 row_mask:0xf bank_mask:0xf\n" \
  "s_waitcnt lgkmcnt(4)\nv_mfma_f32_16x16x32_bf16 v[38:41], v[58:61], v[182:185], v[38:41]\ns_cmp_gt_u32 s62, s40\n" \
  "s_cselect_b32 s52, s52, .Lgen%=-.Lorg%=\nv_cvt_pk_bf16_f32 v92, v74, v76\nv_cvt_pk_bf16_f32 v93, v75, v77\n" \
  "ds_read_b64_tr_b16 v[76:77], v129 offset:16640\nv_mfma_f32_16x16x32_bf16 v[34:37], v[46:49], v[154:157], 0\ns_add_u32 s40, s40, 1\n" \
  "v_cvt_pk_bf16_f32 v94, v66, v68\nv_cvt_pk_bf16_f32 v95, v67, v69\nv_mfma_f32_16x16x32_bf16 v[34:37], v[50:53], v[158:161], v[34:37]\n" \
  "s_cmp_lt_u32 s40, %2\ns_cselect_b32 s52, s52, .Lgen%=-.Lorg%=\nds_read_b64_tr_b16 v[68:69], v129 offset:256\n" \
  "v_fmac_f32_dpp v78, v127, v42 row_newbcast:4 row_mask:0xf bank_mask:0xf\nv_mfma_f32_16x16x32_bf16 v[34:37], v[54:57], v[162:165], v[34:37]\ns_add_u32 s58, s56, s52\n" \
  "s_addc_u32 s59, s57, 0\nv_fmac_f32_dpp v80, v127, v43 row_newbcast:5 row_mask:0xf bank_mask:0xf\nv_fmac_f32_dpp v79, v127, v44 row_newbcast:6 row_mask:0xf bank_mask:0xf\n" \
  "v_mfma_f32_16x16x32_bf16 v[34:37], v[58:61], v[166:169], v[34:37]\ns_lshl_b32 s51, %0, 14\nv_fmac_f32_dpp v81, v127, v45 row_newbcast:7 row_mask:0xf bank_mask:0xf\n" \
  "v_cvt_pk_bf16_f32 v96, v78, v80\nv_cvt_pk_bf16_f32 v97, v79, v81\ns_waitcnt lgkmcnt(5)\n" \
  "v_mfma_f32_16x16x32_bf16 v[208:211], v[86:89], v[102:105], v[208:211]\ns_add_u32 s51, s51, %7\nds_read_b64_tr_b16 v[80:81], v129 offset:16896\n" \
  "v_cvt_pk_bf16_f32 v98, v70, v72\ns_waitcnt lgkmcnt(5)\nv_mfma_f32_16x16x32_bf16 v[224:227], v[86:89], v[106:109], v[224:227]\n" \
  "s_add_u32 s53, s51, 0x10000\ns_setprio 1\nv_cvt_pk_bf16_f32 v99, v71, v73\n" \
  "ds_read_b64_tr_b16 v[72:73], v129 offset:512\nv_mfma_f32_16x16x32_bf16 v[212:215], v[90:93], v[102:105], v[212:215]\nv_fmac_f32_dpp v82, v127, v42 row_newbcast:8 row_mask:0xf bank_mask:0xf\n" \
  "v_fmac_f32_dpp v84, v127, v43 row_newbcast:9 row_mask:0xf bank_mask:0xf\nv_mfma_f32_16x16x32_bf16 v[228:231], v[90:93], v[106:109], v[228:231]\nv_fmac_f32_dpp v83, v127, v44 row_newbcast:10 row_mask:0xf bank_mask:0xf\n" \
  "v_fmac_f32_dpp v85, v127, v45 row_newbcast:11 row_mask:0xf bank_mask:0xf\nv_cvt_pk_bf16_f32 v100, v82, v84\nv_mfma_f32_16x16x32_bf16 v[216:219], v[94:97], v[102:105], v[216:219]\n" \
  "v_cvt_pk_bf16_f32 v101, v83, v85\nds_read_b64_tr_b16 v[84:85], v129 offset:17152\nv_mfma_f32_16x16x32_bf16 v[232:235], v[94:97], v[106:109], v[232:235]\n" \
  "v_add_u32 v128, s47, v136\nds_read_b64 v[130:131], v128\nv_mfma_f32_16x16x32_bf16 v[220:223], v[98:101], v[102:105], v[220:223]\n" \
  "v_add_u32 v128, s46, v135\nds_read_b32 v126, v128\nv_mfma_f32_16x16x32_bf16 v[236:239], v[98:101], v[106:109], v[236:239]\n" \
  "ds_read_b32 v127, v128 offset:192\ns_waitcnt lgkmcnt(8)\nv_lshlrev_b32 v62, 16, v64\n" \
  "v_lshlrev_b32 v63, 16, v65\nv_mfma_f32_16x16x32_bf16 v[42:45], v[46:49], v[186:189], 0\ns_waitcnt lgkmcnt(7)\n" \
  "v_lshlrev_b32 v74, 16, v76\nv_lshlrev_b32 v75, 16, v77\nv_mfma_f32_16x16x32_bf16 v[42:45], v[50:53], v[190:193], v[42:45]\n" \
  "s_setprio 0\ns_waitcnt lgkmcnt(6)\nv_lshlrev_b32 v66, 16, v68\n" \
  "v_lshlrev_b32 v67, 16, v69\nv_mfma_f32_16x16x32_bf16 v[42:45], v[54:57], v[194:197], v[42:45]\ns_waitcnt lgkmcnt(2)\n" \
  "v_xor_b32 v128, v130, v140\nv_add_u32 v129, s50, v137\nv_mfma_f32_16x16x32_bf16 v[42:45], v[58:61], v[198:201], v[42:45]\n" \
  "v_pk_sub_u16 v128, 1, v128 op_sel_hi:[0,1] clamp\nv_lshlrev_b32 v78, 16, v80\nv_lshlrev_b32 v79, 16, v81\n" \
  "v_mfma_f32_16x16x16_bf16 v[46:49], v[112:113], v[202:203], 0\ns_mov_b32 s44, s43\ns_add_u32 s43, s43, 33280\n" \
  "s_cmp_eq_u32 s43, 99840\ns_cselect_b32 s43, 0, s43\nv_pk_mul_lo_u16 v128, v128, v131\n" \
  "ds_write_b32 v129, v128\nv_mfma_f32_16x16x16_bf16 v[50:53], v[112:113], v[204:205], 0\ns_add_u32 s45, s45, 1024\n" \
  "s_add_u32 s46, s46, 384\ns_add_u32 s47, s47, 128\nv_lshlrev_b32 v70, 16, v72\n" \
  "v_lshlrev_b32 v71, 16, v73\nv_mfma_f32_16x16x16_bf16 v[54:57], v[112:113], v[206:207], 0\ns_add_u32 s48, s48, 128\n" \
  "s_xor_b32 s50, s50, 2048\ns_sub_u32 s49, s63, s50\nv_lshlrev_b32 v82, 16, v84\n" \
  "v_lshlrev_b32 v83, 16, v85\nv_add_u32 v128, s43, v132\ns_setpc_b64 s[58:59]\n" \
  ".Ld0%=:\ns_waitcnt lgkmcnt(6)\nv_mfma_f32_16x16x32_bf16 v[38:41], v[46:49], v[170:173], 0\n" \
  "s_setprio 2\nds_read_b64_tr_b16 v[114:115], v139\nds_read_b64_tr_b16 v[116:117], v139 offset:256\n" \
  "ds_read_b64_tr_b16 v[118:119], v139 offset:512\ns_waitcnt lgkmcnt(8)\nv_mfma_f32_16x16x32_bf16 v[38:41], v[50:53], v[174:177], v[38:41]\n" \
  "s_add_u32 s52, s42, .La0%=-.Lorg%=\nds_read_b64_tr_b16 v[120:121], v139 offset:768\nv_cvt_pk_bf16_f32 v90, v62, v64\n" \
  "v_cvt_pk_bf16_f32 v91, v63, v65\ns_waitcnt lgkmcnt(8)\nv_mfma_f32_16x16x32_bf16 v[38:41], v[54:57], v[178:181], v[38:41]\n" \
  "s_and_b32 s62, s54, 0xff\nds_read_b64_tr_b16 v[64:65], v129\nv_fmac_f32_dpp v74, v127, v42 row_newbcast:0 row_mask:0xf bank_mask:0xf\n" \
  "v_fmac_f32_dpp v76, v127, v43 row_newbcast:1 row_mask:0xf bank_mask:0xf\ns_waitcnt lgkmcnt(8)\nv_mfma_f32_16x16x32_bf16 v[38:41], v[58:61], v[182:185], v[38:41]\n" \
  "s_cmp_gt_u32 s62, s40\ns_cselect_b32 s52, s52, .Lgen%=-.Lorg%=\nv_fmac_f32_dpp v75, v127, v44 row_newbcast:2 row_mask:0xf bank_mask:0xf\n" \
  "v_fmac_f32_dpp v77, v127, v45 row_newbcast:3 row_mask:0xf bank_mask:0xf\nv_cvt_pk_bf16_f32 v92, v74, v76\nv_mfma_f32_16x16x32_bf16 v[34:37], v[46:49], v[154:157], 0\n" \
  "s_add_u32 s40, s40, 1\nv_cvt_pk_bf16_f32 v93, v75, v77\nds_read_b64_tr_b16 v[76:77], v129 offset:16640\n" \
  "v_cvt_pk_bf16_f32 v94, v66, v68\nv_mfma_f32_16x16x32_bf16 v[34:37], v[50:53], v[158:161], v[34:37]\ns_cmp_lt_u32 s40, %2\n" \
  "s_cselect_b32 s52, s52, .Lgen%=-.Lorg%=\nv_cvt_pk_bf16_f32 v95, v67, v69\nds_read_b64_tr_b16 v[68:69], v129 offset:256\n" \
  "v_mfma_f32_16x16x32_bf16 v[34:37], v[54:57], v[162:165], v[34:37]\ns_add_u32 s58, s56, s52\ns_addc_u32 s59, s57, 0\n" \
  "v_fmac_f32_dpp v78, v127, v42 row_newbcast:4 row_mask:0xf bank_mask:0xf\nv_fmac_f32_dpp v80, v127, v43 row_newbcast:5 row_mask:0xf bank_mask:0xf\nv_fmac_f32_dpp v79, v127, v44 row_newbcast:6 row_mask:0xf bank_mask:0xf\n" \
  "v_mfma_f32_16x16x32_bf16 v[34:37], v[58:61], v[166:169], v[34:37]\ns_lshl_b32 s51, %0, 14\nv_fmac_f32_dpp v81, v127, v45 row_newbcast:7 row_mask:0xf bank_mask:0xf\n" \
  "v_cvt_pk_bf16_f32 v96, v78, v80\nv_cvt_pk_bf16_f32 v97, v79, v81\ns_waitcnt lgkmcnt(6)\n" \
  "v_mfma_f32_16x16x16_bf16 v[208:211], v[114:115], v[150:151], v[208:211]\ns_add_u32 s51, s51, %7\nds_read_b64_tr_b16 v[80:81], v129 offset:16896\n" \
  "v_cvt_pk_bf16_f32 v98, v70, v72\nv_cvt_pk_bf16_f32 v99, v71, v73\ns_waitcnt lgkmcnt(6)\n" \
  "v_mfma_f32_16x16x16_bf16 v[212:215], v[116:117], v[150:151], v[212:215]\ns_add_u32 s53, s51, 0x10000\nds_read_b64_tr_b16 v[72:73], v129 offset:512\n" \
  "v_fmac_f32_dpp v82, v127, v42 row_newbcast:8 row_mask:0xf bank_mask:0xf\nv_fmac_f32_dpp v84, v127, v43 row_newbcast:9 row_mask:0xf bank_mask:0xf\ns_waitcnt lgkmcnt(6)\n" \
  "v_mfma_f32_16x16x16_bf16 v[216:219], v[118:119], v[150:151], v[216:219]\nv_fmac_f32_dpp v83, v127, v44 row_newbcast:10 row_mask:0xf bank_mask:0xf\nv_fmac_f32_dpp v85, v127, v45 row_newbcast:11 row_mask:0xf bank_mask:0xf\n" \
  "s_waitcnt lgkmcnt(5)\nv_mfma_f32_16x16x16_bf16 v[220:223], v[120:121], v[150:151], v[220:223]\nv_cvt_pk_bf16_f32 v100, v82, v84\n" \
  "v_cvt_pk_bf16_f32 v101, v83, v85\nds_read_b64_tr_b16 v[84:85], v129 offset:17152\nv_mfma_f32_16x16x32_bf16 v[208:211], v[86:89], v[102:105], v[208:211]\n" \
  "v_add_u32 v128, s47, v136\nds_read_b64 v[130:131], v128\nv_add_u32 v128, s46, v135\n" \
  "v_mfma_f32_16x16x32_bf16 v[224:227], v[86:89], v[106:109], v[224:227]\ns_setprio 1\nds_read_b32 v126, v128\n" \
  "ds_read_b32 v127, v128 offset:192\ns_waitcnt lgkmcnt(8)\nv_lshlrev_b32 v62, 16, v64\n" \
  "v_mfma_f32_16x16x32_bf16 v[212:215], v[90:93], v[102:105], v[212:215]\nv_lshlrev_b32 v63, 16, v65\ns_waitcnt lgkmcnt(7)\n" \
  "v_lshlrev_b32 v74, 16, v76\nv_lshlrev_b32 v75, 16, v77\nv_mfma_f32_16x16x32_bf16 v[228:231], v[90:93], v[106:109], v[228:231]\n" \
  "s_waitcnt lgkmcnt(6)\nv_lshlrev_b32 v66, 16, v68\nv_lshlrev_b32 v67, 16, v69\n" \
  "s_waitcnt lgkmcnt(2)\nv_xor_b32 v128, v130, v140\nv_mfma_f32_16x16x32_bf16 v[216:219], v[94:97], v[102:105], v[216:219]\n" \
  "v_add_u32 v129, s50, v137\nv_pk_sub_u16 v128, 1, v128 op_sel_hi:[0,1] clamp\nv_mfma_f32_16x16x32_bf16 v[232:235], v[94:97], v[106:109], v[232:235]\n" \
  "v_lshlrev_b32 v78, 16, v80\nv_lshlrev_b32 v79, 16, v81\nv_pk_mul_lo_u16 v128, v128, v131\n" \
  "v_mfma_f32_16x16x32_bf16 v[220:223], v[98:101], v[102:105], v[220:223]\nds_write_b32 v129, v128\nv_lshlrev_b32 v70, 16, v72\n" \
  "v_lshlrev_b32 v71, 16, v73\nv_mfma_f32_16x16x32_bf16 v[236:239], v[98:101], v[106:109], v[236:239]\nv_lshlrev_b32 v82, 16, v84\n" \
  "v_lshlrev_b32 v83, 16, v85\nv_cvt_pk_bf16_f32 v102, v208, v209\nv_mfma_f32_16x16x32_bf16 v[42:45], v[46:49], v[186:189], 0\n" \
  "v_cvt_pk_bf16_f32 v103, v210, v211\nds_write_b64 v138, v[102:103] offset:0\nv_mov_b64 v[208:209], 0\n" \
  "v_mfma_f32_16x16x32_bf16 v[42:45], v[50:53], v[190:193], v[42:45]\ns_setprio 0\nv_mov_b64 v[210:211], 0\n" \
  "v_cvt_pk_bf16_f32 v104, v212, v213\nv_mfma_f32_16x16x32_bf16 v[42:45], v[54:57], v[194:197], v[42:45]\nv_cvt_pk_bf16_f32 v105, v214, v215\n" \
  "ds_write_b64 v138, v[104:105] offset:256\nv_mov_b64 v[212:213], 0\nv_mfma_f32_16x16x32_bf16 v[42:45], v[58:61], v[198:201], v[42:45]\n" \
  "v_mov_b64 v[214:215], 0\nv_cvt_pk_bf16_f32 v106, v216, v217\nv_cvt_pk_bf16_f32 v107, v218, v219\n" \
  "v_mfma_f32_16x16x16_bf16 v[46:49], v[112:113], v[202:203], 0\ns_mov_b32 s44, s43\ns_add_u32 s43, s43, 33280\n" \
  "s_cmp_eq_u32 s43, 99840\ns_cselect_b32 s43, 0, s43\nds_write_b64 v138, v[106:107] offset:512\n" \
  "v_mov_b64 v[216:217], 0\nv_mov_b64 v[218:219], 0\nv_mfma_f32_16x16x16_bf16 v[50:53], v[112:113], v[204:205], 0\n" \
  "s_add_u32 s45, s45, 1024\ns_add_u32 s46, s46, 384\ns_add_u32 s47, s47, 128\n" \
  "v_cvt_pk_bf16_f32 v108, v220, v221\nv_cvt_pk_bf16_f32 v109, v222, v223\nds_write_b64 v138, v[108:109] offset:768\n" \
  "v_mfma_f32_16x16x16_bf16 v[54:57], v[112:113], v[206:207], 0\ns_add_u32 s48, s48, 128\ns_xor_b32 s50, s50, 2048\n" \
  "s_sub_u32 s49, s63, s50\nv_mov_b64 v[220:221], 0\nv_mov_b64 v[222:223], 0\n" \
  "v_add_u32 v128, s43, v132\ns_setpc_b64 s[58:59]\n.Ld1%=:\n" \
  "s_waitcnt lgkmcnt(6)\nv_mfma_f32_16x16x32_bf16 v[38:41], v[46:49], v[170:173], 0\ns_setprio 2\n" \
  "ds_read_b64_tr_b16 v[114:115], v139 offset:16640\nds_read_b64_tr_b16 v[116:117], v139 offset:16896\nds_read_b64_tr_b16 v[118:119], v139 offset:17152\n" \
  "s_waitcnt lgkmcnt(8)\nv_mfma_f32_16x16x32_bf16 v[38:41], v[50:53], v[174:177], v[38:41]\ns_add_u32 s52, s42, .La1%=-.Lorg%=\n" \
  "ds_read_b64_tr_b16 v[120:121], v139 offset:17408\nv_cvt_pk_bf16_f32 v90, v62, v64\nv_cvt_pk_bf16_f32 v91, v63, v65\n" \
  "s_waitcnt lgkmcnt(8)\nv_mfma_f32_16x16x32_bf16 v[38:41], v[54:57], v[178:181], v[38:41]\ns_and_b32 s62, s54, 0xff\n" \
  "ds_read_b64_tr_b16 v[64:65], v129\nv_fmac_f32_dpp v74, v127, v42 row_newbcast:0 row_mask:0xf bank_mask:0xf\nv_fmac_f32_dpp v76, v127, v43 row_newbcast:1 row_mask:0xf bank_mask:0xf\n" \
  "s_waitcnt lgkmcnt(8)\nv_mfma_f32_16x16x32_bf16 v[38:41], v[58:61], v[182:185], v[38:41]\ns_cmp_gt_u32 s62, s40\n" \
  "s_cselect_b32 s52, s52, .Lgen%=-.Lorg%=\nv_fmac_f32_dpp v75, v127, v44 row_newbcast:2 row_mask:0xf bank_mask:0xf\nv_fmac_f32_dpp v77, v127, v45 row_newbcast:3 row_mask:0xf bank_mask:0xf\n" \
  "v_cvt_pk_bf16_f32 v92, v74, v76\nv_mfma_f32_16x16x32_bf16 v[34:37], v[46:49], v[154:157], 0\ns_add_u32 s40, s40, 1\n" \
  "v_cvt_pk_bf16_f32 v93, v75, v77\nds_read_b64_tr_b16 v[76:77], v129 offset:16640\nv_cvt_pk_bf16_f32 v94, v66, v68\n" \
  "v_mfma_f32_16x16x32_bf16 v[34:37], v[50:53], v[158:161], v[34:37]\ns_cmp_lt_u32 s40, %2\ns_cselect_b32 s52, s52, .Lgen%=-.Lorg%=\n" \
  "v_cvt_pk_bf16_f32 v95, v67, v69\nds_read_b64_tr_b16 v[68:69], v129 offset:256\nv_mfma_f32_16x16x32_bf16 v[34:37], v[54:57], v[162:165], v[34:37]\n" \
  "s_add_u32 s58, s56, s52\ns_addc_u32 s59, s57, 0\nv_fmac_f32_dpp v78, v127, v42 row_newbcast:4 row_mask:0xf bank_mask:0xf\n" \
  "v_fmac_f32_dpp v80, v127, v43 row_newbcast:5 row_mask:0xf bank_mask:0xf\nv_fmac_f32_dpp v79, v127, v44 row_newbcast:6 row_mask:0xf bank_mask:0xf\nv_mfma_f32_16x16x32_bf16 v[34:37], v[58:61], v[166:169], v[34:37]\n" \
  "s_lshl_b32 s51, %0, 14\nv_fmac_f32_dpp v81, v127, v45 row_newbcast:7 row_mask:0xf bank_mask:0xf\nv_cvt_pk_bf16_f32 v96, v78, v80\n" \
  "v_cvt_pk_bf16_f32 v97, v79, v81\ns_waitcnt lgkmcnt(6)\nv_mfma_f32_16x16x16_bf16 v[224:227], v[114:115], v[150:151], v[224:227]\n" \
  "s_add_u32 s51, s51, %7\nds_read_b64_tr_b16 v[80:81], v129 offset:16896\nv_cvt_pk_bf16_f32 v98, v70, v72\n" \
  "v_cvt_pk_bf16_f32 v99, v71, v73\ns_waitcnt lgkmcnt(6)\nv_mfma_f32_16x16x16_bf16 v[228:231], v[116:117], v[150:151], v[228:231]\n" \
  "s_add_u32 s53, s51, 0x10000\nds_read_b64_tr_b16 v[72:73], v129 offset:512\nv_fmac_f32_dpp v82, v127, v42 row_newbcast:8 row_mask:0xf bank_mask:0xf\n" \
  "v_fmac_f32_dpp v84, v127, v43 row_newbcast:9 row_mask:0xf bank_mask:0xf\ns_waitcnt lgkmcnt(6)\nv_mfma_f32_16x16x16_bf16 v[232:235], v[118:119], v[150:151], v[232:235]\n" \
  "v_fmac_f32_dpp v83, v127, v44 row_newbcast:10 row_mask:0xf bank_mask:0xf\nv_fmac_f32_dpp v85, v127, v45 row_newbcast:11 row_mask:0xf bank_mask:0xf\ns_waitcnt lgkmcnt(5)\n" \
  "v_mfma_f32_16x16x16_bf16 v[236:239], v[120:121], v[150:151], v[236:239]\nv_cvt_pk_bf16_f32 v100, v82, v84\nv_cvt_pk_bf16_f32 v101, v83, v85\n" \
  "ds_read_b64_tr_b16 v[84:85], v129 offset:17152\nv_mfma_f32_16x16x32_bf16 v[208:211], v[86:89], v[102:105], v[208:211]\nv_add_u32 v128, s47, v136\n" \
  "ds_read_b64 v[130:131], v128\nv_add_u32 v128, s46, v135\nv_mfma_f32_16x16x32_bf16 v[224:227], v[86:89], v[106:109], v[224:227]\n" \
  "s_setprio 1\nds_read_b32 v126, v128\nds_read_b32 v127, v128 offset:192\n" \
  "s_waitcnt lgkmcnt(8)\nv_lshlrev_b32 v62, 16, v64\nv_mfma_f32_16x16x32_bf16 v[212:215], v[90:93], v[102:105], v[212:215]\n" \
  "v_lshlrev_b32 v63, 16, v65\ns_waitcnt lgkmcnt(7)\nv_lshlrev_b32 v74, 16, v76\n" \
  "v_lshlrev_b32 v75, 16, v77\nv_mfma_f32_16x16x32_bf16 v[228:231], v[90:93], v[106:109], v[228:231]\ns_waitcnt lgkmcnt(6)\n" \
  "v_lshlrev_b32 v66, 16, v68\nv_lshlrev_b32 v67, 16, v69\ns_waitcnt lgkmcnt(2)\n" \
  "v_xor_b32 v128, v130, v140\nv_mfma_f32_16x16x32_bf16 v[216:219], v[94:97], v[102:105], v[216:219]\nv_add_u32 v129, s50, v137\n" \
  "v_pk_sub_u16 v128, 1, v128 op_sel_hi:[0,1] clamp\nv_mfma_f32_16x16x32_bf16 v[232:235], v[94:97], v[106:109], v[232:235]\nv_lshlrev_b32 v78, 16, v80\n" \
  "v_lshlrev_b32 v79, 16, v81\nv_pk_mul_lo_u16 v128, v128, v131\nv_mfma_f32_16x16x32_bf16 v[220:223], v[98:101], v[102:105], v[220:223]\n" \
  "ds_write_b32 v129, v128\nv_lshlrev_b32 v70, 16, v72\nv_lshlrev_b32 v71, 16, v73\n" \
  "v_mfma_f32_16x16x32_bf16 v[236:239], v[98:101], v[106:109], v[236:239]\nv_lshlrev_b32 v82, 16, v84\nv_lshlrev_b32 v83, 16, v85\n" \
  "v_cvt_pk_bf16_f32 v102, v224, v225\nv_mfma_f32_16x16x32_bf16 v[42:45], v[46:49], v[186:189], 0\nv_cvt_pk_bf16_f32 v103, v226, v227\n" \
  "ds_write_b64 v138, v[102:103] offset:16640\nv_mov_b64 v[224:225], 0\nv_mfma_f32_16x16x32_bf16 v[42:45], v[50:53], v[190:193], v[42:45]\n" \
  "s_setprio 0\nv_mov_b64 v[226:227], 0\nv_cvt_pk_bf16_f32 v104, v228, v229\n" \
  "v_mfma_f32_16x16x32_bf16 v[42:45], v[54:57], v[194:197], v[42:45]\nv_cvt_pk_bf16_f32 v105, v230, v231\nds_write_b64 v138, v[104:105] offset:16896\n" \
  "v_mov_b64 v[228:229], 0\nv_mfma_f32_16x16x32_bf16 v[42:45], v[58:61], v[198:201], v[42:45]\nv_mov_b64 v[230:231], 0\n" \
  "v_cvt_pk_bf16_f32 v106, v232, v233\nv_cvt_pk_bf16_f32 v107, v234, v235\nv_mfma_f32_16x16x16_bf16 v[46:49], v[112:113], v[202:203], 0\n" \
  "s_mov_b32 s44, s43\ns_add_u32 s43, s43, 33280\ns_cmp_eq_u32 s43, 99840\n" \
  "s_cselect_b32 s43, 0, s43\nds_write_b64 v138, v[106:107] offset:17152\nv_mov_b64 v[232:233], 0\n" \
  "v_mov_b64 v[234:235], 0\nv_mfma_f32_16x16x16_bf16 v[50:53], v[112:113], v[204:205], 0\ns_add_u32 s45, s45, 1024\n" \
  "s_add_u32 s46, s46, 384\ns_add_u32 s47, s47, 128\nv_cvt_pk_bf16_f32 v108, v236, v237\n" \
  "v_cvt_pk_bf16_f32 v109, v238, v239\nds_write_b64 v138, v[108:109] offset:17408\nv_mfma_f32_16x16x16_bf16 v[54:57], v[112:113], v[206:207], 0\n" \
  "s_add_u32 s48, s48, 128\ns_xor_b32 s50, s50, 2048\ns_sub_u32 s49, s63, s50\n" \
  "v_mov_b64 v[236:237], 0\nv_mov_b64 v[238:239], 0\nv_add_u32 v128, s43, v132\n" \
  "s_setpc_b64 s[58:59]\n.Lzero%=:\ns_add_u32 m0, %10, 0\n" \
  "s_nop 0\nbuffer_load_dwordx4 v134, %4, %7 offen lds\ns_add_u32 m0, %10, 16\n" \
  "s_nop 0\nbuffer_load_dwordx4 v134, %4, %7 offen offset:1024 lds\ns_add_u32 m0, %10, 16640\n" \
  "s_nop 0\nbuffer_load_dwordx4 v134, %4, s61 offen lds\ns_add_u32 m0, %10, 16656\n" \
  "s_nop 0\nbuffer_load_dwordx4 v134, %4, s61 offen offset:1024 lds\ns_mov_b32 s40, 1\n" \
  ".Lgen1%=:\ns_add_u32 s60, %7, 0x8000\ns_add_u32 s61, %7, 0xc000\n" \
  "buffer_load_dwordx4 v[240:243], v134, %4, s60 offen\nbuffer_load_dwordx4 v[244:247], v134, %4, s60 offen offset:1024\nbuffer_load_dwordx4 v[248:251], v134, %4, s61 offen\n" \
  "buffer_load_dwordx4 v[252:255], v134, %4, s61 offen offset:1024\ns_mov_b32 s65, 0x7fff\ns_mov_b32 s42, 0\n" \
  ".Lgen%=:\ns_and_b32 s62, s54, 0xff\ns_cmp_lt_u32 s62, s40\n" \
  "s_cbranch_scc1 .Lfl%=\ns_cmp_gt_u32 s40, %2\ns_cbranch_scc1 .Lend%=\n" \
  "s_add_u32 s52, %0, 1\ns_cmp_lt_u32 s52, %1\ns_cbranch_scc1 .Lslow%=\n" \
  "s_lshl_b32 s51, %0, 14\ns_add_u32 s51, s51, %7\ns_add_u32 s53, s51, 0x10000\n" \
  "s_cmp_eq_u32 s40, %2\ns_cbranch_scc1 .Ltail%=\ns_cmp_eq_u32 %0, %1\n" \
  "s_cbranch_scc1 .Lan%=\ns_bitcmp1_b32 %0, 0\ns_cbranch_scc1 .La1%=\n" \
  "s_branch .La0%=\n.Ltail%=:\ns_add_u32 s40, s40, 1\n" \
  "s_mov_b32 s41, %0\ns_cmp_eq_u32 s42, 4\ns_cbranch_scc1 .Lt4%=\n" \
  "s_waitcnt vmcnt(0)\n.Lt4%=:\ns_waitcnt vmcnt(4) lgkmcnt(0)\n" \
  "s_barrier\ns_cmp_eq_u32 %0, %1\ns_cbranch_scc1 .Ltn%=\n" \
  "s_add_u32 %0, %0, 1\ns_bitcmp0_b32 %0, 0\ns_cbranch_scc1 .Lt1%=\n" \
  "v_mul_f32 v6, v6, v18\nv_mul_f32 v7, v7, v19\nv_mul_f32 v8, v8, v20\n" \
  "v_mul_f32 v9, v9, v21\nv_mul_f32 v10, v10, v22\nv_mul_f32 v11, v11, v23\n" \
  "v_mul_f32 v12, v12, v24\nv_mul_f32 v13, v13, v25\nv_mul_f32 v14, v14, v26\n" \
  "v_mul_f32 v15, v15, v27\nv_mul_f32 v16, v16, v28\nv_mul_f32 v17, v17, v29\n" \
  "v_and_b32 v64, v64, v147\nv_and_b32 v65, v65, v147\nv_and_b32 v68, v68, v147\n" \
  "v_and_b32 v69, v69, v147\nds_read_b128 v[114:117], v146\nv_and_b32 v72, v72, v147\n" \
  "v_and_b32 v73, v73, v147\nv_and_b32 v76, v76, v147\nv_and_b32 v77, v77, v147\n" \
  "ds_read_b128 v[118:121], v146 offset:1040\nv_and_b32 v80, v80, v147\nv_and_b32 v81, v81, v147\n" \
  "v_and_b32 v84, v84, v147\nv_and_b32 v85, v85, v147\nv_mul_f32 v38, v38, v50\n" \
  "v_mul_f32 v39, v39, v51\nv_mul_f32 v40, v40, v52\nv_mul_f32 v41, v41, v53\n" \
  "v_mul_f32 v62, v62, v10\nv_mul_f32 v64, v64, v11\nv_mul_f32 v63, v63, v12\n" \
  "v_mul_f32 v65, v65, v13\nv_mul_f32 v34, v34, v46\nv_mul_f32 v35, v35, v47\n" \
  "v_mul_f32 v36, v36, v48\nv_mul_f32 v37, v37, v49\nv_mul_f32 v66, v66, v10\n" \
  "v_mul_f32 v68, v68, v11\nv_mul_f32 v67, v67, v12\nv_mul_f32 v69, v69, v13\n" \
  "v_mul_f32 v42, v42, v54\nv_mul_f32 v43, v43, v55\nv_mul_f32 v44, v44, v56\n" \
  "v_mul_f32 v45, v45, v57\ns_waitcnt lgkmcnt(1)\nbuffer_store_dwordx4 v[114:117], v134, %5, s51 offen nt\n" \
  "v_mul_f32 v70, v70, v10\nv_mul_f32 v72, v72, v11\nv_mul_f32 v71, v71, v12\n" \
  "v_mul_f32 v73, v73, v13\nv_mul_f32 v74, v74, v38\nv_mul_f32 v76, v76, v39\n" \
  "v_mul_f32 v75, v75, v40\nv_mul_f32 v77, v77, v41\ns_waitcnt lgkmcnt(0)\n" \
  "buffer_store_dwordx4 v[118:121], v134, %5, s51 offen offset:1024 nt\nv_mul_f32 v78, v78, v38\nv_mul_f32 v80, v80, v39\n" \
  "v_mul_f32 v79, v79, v40\nv_mul_f32 v81, v81, v41\nv_mul_f32 v82, v82, v38\n" \
  "v_mul_f32 v84, v84, v39\nv_mul_f32 v83, v83, v40\nv_mul_f32 v85, v85, v41\n" \
  "ds_write_b128 v146, v[240:243] offset:0\nbuffer_load_dwordx4 v[240:243], v134, %4, s53 offen\nds_write_b128 v146, v[244:247] offset:1040\n" \
  "buffer_load_dwordx4 v[244:247], v134, %4, s53 offen offset:1024\nv_cvt_pk_bf16_f32 v86, v6, v7\nv_cvt_pk_bf16_f32 v87, v8, v9\n" \
  "v_fmac_f32_dpp v62, v126, v14 row_newbcast:0 row_mask:0xf bank_mask:0xf\nv_fmac_f32_dpp v64, v126, v15 row_newbcast:1 row_mask:0xf bank_mask:0xf\nv_fmac_f32_dpp v63, v126, v16 row_newbcast:2 row_mask:0xf bank_mask:0xf\n" \
  "v_fmac_f32_dpp v65, v126, v17 row_newbcast:3 row_mask:0xf bank_mask:0xf\nv_fmac_f32_dpp v66, v126, v14 row_newbcast:4 row_mask:0xf bank_mask:0xf\nv_fmac_f32_dpp v68, v126, v15 row_newbcast:5 row_mask:0xf bank_mask:0xf\n" \
  "v_fmac_f32_dpp v67, v126, v16 row_newbcast:6 row_mask:0xf bank_mask:0xf\nv_fmac_f32_dpp v69, v126, v17 row_newbcast:7 row_mask:0xf bank_mask:0xf\nv_fmac_f32_dpp v70, v126, v14 row_newbcast:8 row_mask:0xf bank_mask:0xf\n" \
  "v_fmac_f32_dpp v72, v126, v15 row_newbcast:9 row_mask:0xf bank_mask:0xf\nv_fmac_f32_dpp v71, v126, v16 row_newbcast:10 row_mask:0xf bank_mask:0xf\nv_fmac_f32_dpp v73, v126, v17 row_newbcast:11 row_mask:0xf bank_mask:0xf\n" \
  "v_add_u32 v128, s49, v134\nds_read_b128 v[102:105], v128\nds_read_b128 v[106:109], v128 offset:1024\n" \
  "v_cvt_pk_bf16_f32 v88, v34, v35\nv_cvt_pk_bf16_f32 v89, v36, v37\ns_waitcnt lgkmcnt(1)\n" \
  "s_nop 0\nv_mfma_f32_16x16x32_bf16 v[208:211], v[86:89], v[102:105], v[208:211]\nv_cvt_pk_bf16_f32 v90, v62, v64\n" \
  "v_cvt_pk_bf16_f32 v91, v63, v65\nv_fmac_f32_dpp v74, v127, v42 row_newbcast:0 row_mask:0xf bank_mask:0xf\ns_waitcnt lgkmcnt(0)\n" \
  "v_mfma_f32_16x16x32_bf16 v[224:227], v[86:89], v[106:109], v[224:227]\nv_fmac_f32_dpp v76, v127, v43 row_newbcast:1 row_mask:0xf bank_mask:0xf\nv_fmac_f32_dpp v75, v127, v44 row_newbcast:2 row_mask:0xf bank_mask:0xf\n" \
  "v_fmac_f32_dpp v77, v127, v45 row_newbcast:3 row_mask:0xf bank_mask:0xf\nv_cvt_pk_bf16_f32 v92, v74, v76\nv_cvt_pk_bf16_f32 v93, v75, v77\n" \
  "s_nop 1\nv_mfma_f32_16x16x32_bf16 v[212:215], v[90:93], v[102:105], v[212:215]\nv_cvt_pk_bf16_f32 v94, v66, v68\n" \
  "v_mfma_f32_16x16x32_bf16 v[228:231], v[90:93], v[106:109], v[228:231]\nv_cvt_pk_bf16_f32 v95, v67, v69\nv_fmac_f32_dpp v78, v127, v42 row_newbcast:4 row_mask:0xf bank_mask:0xf\n" \
  "v_fmac_f32_dpp v80, v127, v43 row_newbcast:5 row_mask:0xf bank_mask:0xf\nv_fmac_f32_dpp v79, v127, v44 row_newbcast:6 row_mask:0xf bank_mask:0xf\nv_fmac_f32_dpp v81, v127, v45 row_newbcast:7 row_mask:0xf bank_mask:0xf\n" \
  "v_cvt_pk_bf16_f32 v96, v78, v80\nv_cvt_pk_bf16_f32 v97, v79, v81\ns_nop 1\n" \
  "v_mfma_f32_16x16x32_bf16 v[216:219], v[94:97], v[102:105], v[216:219]\nv_mfma_f32_16x16x32_bf16 v[232:235], v[94:97], v[106:109], v[232:235]\nv_cvt_pk_bf16_f32 v98, v70, v72\n" \
  "v_cvt_pk_bf16_f32 v99, v71, v73\nv_fmac_f32_dpp v82, v127, v42 row_newbcast:8 row_mask:0xf bank_mask:0xf\nv_fmac_f32_dpp v84, v127, v43 row_newbcast:9 row_mask:0xf bank_mask:0xf\n" \
  "v_fmac_f32_dpp v83, v127, v44 row_newbcast:10 row_mask:0xf bank_mask:0xf\nv_fmac_f32_dpp v85, v127, v45 row_newbcast:11 row_mask:0xf bank_mask:0xf\nv_cvt_pk_bf16_f32 v100, v82, v84\n" \
  "v_cvt_pk_bf16_f32 v101, v83, v85\ns_nop 1\nv_mfma_f32_16x16x32_bf16 v[220:223], v[98:101], v[102:105], v[220:223]\n" \
  "v_mfma_f32_16x16x32_bf16 v[236:239], v[98:101], v[106:109], v[236:239]\ns_nop 7\ns_branch .Lgen%=\n" \
  ".Lt1%=:\nv_mul_f32 v6, v6, v18\nv_mul_f32 v7, v7, v19\n" \
  "v_mul_f32 v8, v8, v20\nv_mul_f32 v9, v9, v21\nv_mul_f32 v10, v10, v22\n" \
  "v_mul_f32 v11, v11, v23\nv_mul_f32 v12, v12, v24\nv_mul_f32 v13, v13, v25\n" \
  "v_mul_f32 v14, v14, v26\nv_mul_f32 v15, v15, v27\nv_mul_f32 v16, v16, v28\n" \
  "v_mul_f32 v17, v17, v29\nv_and_b32 v64, v64, v147\nv_and_b32 v65, v65, v147\n" \
  "v_and_b32 v68, v68, v147\nv_and_b32 v69, v69, v147\nds_read_b128 v[114:117], v146 offset:16640\n" \
  "v_and_b32 v72, v72, v147\nv_and_b32 v73, v73, v147\nv_and_b32 v76, v76, v147\n" \
  "v_and_b32 v77, v77, v147\nds_read_b128 v[118:121], v146 offset:17680\nv_and_b32 v80, v80, v147\n" \
  "v_and_b32 v81, v81, v147\nv_and_b32 v84, v84, v147\nv_and_b32 v85, v85, v147\n" \
  "v_mul_f32 v38, v38, v50\nv_mul_f32 v39, v39, v51\nv_mul_f32 v40, v40, v52\n" \
  "v_mul_f32 v41, v41, v53\nv_mul_f32 v62, v62, v10\nv_mul_f32 v64, v64, v11\n" \
  "v_mul_f32 v63, v63, v12\nv_mul_f32 v65, v65, v13\nv_mul_f32 v34, v34, v46\n" \
  "v_mul_f32 v35, v35, v47\nv_mul_f32 v36, v36, v48\nv_mul_f32 v37, v37, v49\n" \
  "v_mul_f32 v66, v66, v10\nv_mul_f32 v68, v68, v11\nv_mul_f32 v67, v67, v12\n" \
  "v_mul_f32 v69, v69, v13\nv_mul_f32 v42, v42, v54\nv_mul_f32 v43, v43, v55\n" \
  "v_mul_f32 v44, v44, v56\nv_mul_f32 v45, v45, v57\ns_waitcnt lgkmcnt(1)\n" \
  "buffer_store_dwordx4 v[114:117], v134, %5, s51 offen nt\nv_mul_f32 v70, v70, v10\nv_mul_f32 v72, v72, v11\n" \
  "v_mul_f32 v71, v71, v12\nv_mul_f32 v73, v73, v13\nv_mul_f32 v74, v74, v38\n" \
  "v_mul_f32 v76, v76, v39\nv_mul_f32 v75, v75, v40\nv_mul_f32 v77, v77, v41\n" \
  "s_waitcnt lgkmcnt(0)\nbuffer_store_dwordx4 v[118:121], v134, %5, s51 offen offset:1024 nt\nv_mul_f32 v78, v78, v38\n" \
  "v_mul_f32 v80, v80, v39\nv_mul_f32 v79, v79, v40\nv_mul_f32 v81, v81, v41\n" \
  "v_mul_f32 v82, v82, v38\nv_mul_f32 v84, v84, v39\nv_mul_f32 v83, v83, v40\n" \
  "v_mul_f32 v85, v85, v41\nds_write_b128 v146, v[248:251] offset:16640\nbuffer_load_dwordx4 v[248:251], v134, %4, s53 offen\n" \
  "ds_write_b128 v146, v[252:255] offset:17680\nbuffer_load_dwordx4 v[252:255], v134, %4, s53 offen offset:1024\nv_cvt_pk_bf16_f32 v86, v6, v7\n" \
  "v_cvt_pk_bf16_f32 v87, v8, v9\nv_fmac_f32_dpp v62, v126, v14 row_newbcast:0 row_mask:0xf bank_mask:0xf\nv_fmac_f32_dpp v64, v126, v15 row_newbcast:1 row_mask:0xf bank_mask:0xf\n" \
  "v_fmac_f32_dpp v63, v126, v16 row_newbcast:2 row_mask:0xf bank_mask:0xf\nv_fmac_f32_dpp v65, v126, v17 row_newbcast:3 row_mask:0xf bank_mask:0xf\nv_fmac_f32_dpp v66, v126, v14 row_newbcast:4 row_mask:0xf bank_mask:0xf\n" \
  "v_fmac_f32_dpp v68, v126, v15 row_newbcast:5 row_mask:0xf bank_mask:0xf\nv_fmac_f32_dpp v67, v126, v16 row_newbcast:6 row_mask:0xf bank_mask:0xf\nv_fmac_f32_dpp v69, v126, v17 row_newbcast:7 row_mask:0xf bank_mask:0xf\n" \
  "v_fmac_f32_dpp v70, v126, v14 row_newbcast:8 row_mask:0xf bank_mask:0xf\nv_fmac_f32_dpp v72, v126, v15 row_newbcast:9 row_mask:0xf bank_mask:0xf\nv_fmac_f32_dpp v71, v126, v16 row_newbcast:10 row_mask:0xf bank_mask:0xf\n" \
  "v_fmac_f32_dpp v73, v126, v17 row_newbcast:11 row_mask:0xf bank_mask:0xf\nv_add_u32 v128, s49, v134\nds_read_b128 v[102:105], v128\n" \
  "ds_read_b128 v[106:109], v128 offset:1024\nv_cvt_pk_bf16_f32 v88, v34, v35\nv_cvt_pk_bf16_f32 v89, v36, v37\n" \
  "s_waitcnt lgkmcnt(1)\ns_nop 0\nv_mfma_f32_16x16x32_bf16 v[208:211], v[86:89], v[102:105], v[208:211]\n" \
  "v_cvt_pk_bf16_f32 v90, v62, v64\nv_cvt_pk_bf16_f32 v91, v63, v65\nv_fmac_f32_dpp v74, v127, v42 row_newbcast:0 row_mask:0xf bank_mask:0xf\n" \
  "s_waitcnt lgkmcnt(0)\nv_mfma_f32_16x16x32_bf16 v[224:227], v[86:89], v[106:109], v[224:227]\nv_fmac_f32_dpp v76, v127, v43 row_newbcast:1 row_mask:0xf bank_mask:0xf\n" \
  "v_fmac_f32_dpp v75, v127, v44 row_newbcast:2 row_mask:0xf bank_mask:0xf\nv_fmac_f32_dpp v77, v127, v45 row_newbcast:3 row_mask:0xf bank_mask:0xf\nv_cvt_pk_bf16_f32 v92, v74, v76\n" \
  "v_cvt_pk_bf16_f32 v93, v75, v77\ns_nop 1\nv_mfma_f32_16x16x32_bf16 v[212:215], v[90:93], v[102:105], v[212:215]\n" \
  "v_cvt_pk_bf16_f32 v94, v66, v68\nv_mfma_f32_16x16x32_bf16 v[228:231], v[90:93], v[106:109], v[228:231]\nv_cvt_pk_bf16_f32 v95, v67, v69\n" \
  "v_fmac_f32_dpp v78, v127, v42 row_newbcast:4 row_mask:0xf bank_mask:0xf\nv_fmac_f32_dpp v80, v127, v43 row_newbcast:5 row_mask:0xf bank_mask:0xf\nv_fmac_f32_dpp v79, v127, v44 row_newbcast:6 row_mask:0xf bank_mask:0xf\n" \
  "v_fmac_f32_dpp v81, v127, v45 row_newbcast:7 row_mask:0xf bank_mask:0xf\nv_cvt_pk_bf16_f32 v96, v78, v80\nv_cvt_pk_bf16_f32 v97, v79, v81\n" \
  "s_nop 1\nv_mfma_f32_16x16x32_bf16 v[216:219], v[94:97], v[102:105], v[216:219]\nv_mfma_f32_16x16x32_bf16 v[232:235], v[94:97], v[106:109], v[232:235]\n" \
  "v_cvt_pk_bf16_f32 v98, v70, v72\nv_cvt_pk_bf16_f32 v99, v71, v73\nv_fmac_f32_dpp v82, v127, v42 row_newbcast:8 row_mask:0xf bank_mask:0xf\n" \
  "v_fmac_f32_dpp v84, v127, v43 row_newbcast:9 row_mask:0xf bank_mask:0xf\nv_fmac_f32_dpp v83, v127, v44 row_newbcast:10 row_mask:0xf bank_mask:0xf\nv_fmac_f32_dpp v85, v127, v45 row_newbcast:11 row_mask:0xf bank_mask:0xf\n" \
  "v_cvt_pk_bf16_f32 v100, v82, v84\nv_cvt_pk_bf16_f32 v101, v83, v85\ns_nop 1\n" \
  "v_mfma_f32_16x16x32_bf16 v[220:223], v[98:101], v[102:105], v[220:223]\nv_mfma_f32_16x16x32_bf16 v[236:239], v[98:101], v[106:109], v[236:239]\ns_nop 7\n" \
  "s_branch .Lgen%=\n.Ltn%=:\nv_mul_f32 v6, v6, v18\n" \
  "v_mul_f32 v7, v7, v19\nv_mul_f32 v8, v8, v20\nv_mul_f32 v9, v9, v21\n" \
  "v_mul_f32 v10, v10, v22\nv_mul_f32 v11, v11, v23\nv_mul_f32 v12, v12, v24\n" \
  "v_mul_f32 v13, v13, v25\nv_mul_f32 v14, v14, v26\nv_mul_f32 v15, v15, v27\n" \
  "v_mul_f32 v16, v16, v28\nv_mul_f32 v17, v17, v29\nv_and_b32 v64, v64, v147\n" \
  "v_and_b32 v65, v65, v147\nv_and_b32 v68, v68, v147\nv_and_b32 v69, v69, v147\n" \
  "v_and_b32 v72, v72, v147\nv_and_b32 v73, v73, v147\nv_and_b32 v76, v76, v147\n" \
  "v_and_b32 v77, v77, v147\nv_and_b32 v80, v80, v147\nv_and_b32 v81, v81, v147\n" \
  "v_and_b32 v84, v84, v147\nv_and_b32 v85, v85, v147\nv_mul_f32 v38, v38, v50\n" \
  "v_mul_f32 v39, v39, v51\nv_mul_f32 v40, v40, v52\nv_mul_f32 v41, v41, v53\n" \
  "v_mul_f32 v62, v62, v10\nv_mul_f32 v64, v64, v11\nv_mul_f32 v63, v63, v12\n" \
  "v_mul_f32 v65, v65, v13\nv_mul_f32 v34, v34, v46\nv_mul_f32 v35, v35, v47\n" \
  "v_mul_f32 v36, v36, v48\nv_mul_f32 v37, v37, v49\nv_mul_f32 v66, v66, v10\n" \
  "v_mul_f32 v68, v68, v11\nv_mul_f32 v67, v67, v12\nv_mul_f32 v69, v69, v13\n" \
  "v_mul_f32 v42, v42, v54\nv_mul_f32 v43, v43, v55\nv_mul_f32 v44, v44, v56\n" \
  "v_mul_f32 v45, v45, v57\nv_mul_f32 v70, v70, v10\nv_mul_f32 v72, v72, v11\n" \
  "v_mul_f32 v71, v71, v12\nv_mul_f32 v73, v73, v13\nv_mul_f32 v74, v74, v38\n" \
  "v_mul_f32 v76, v76, v39\nv_mul_f32 v75, v75, v40\nv_mul_f32 v77, v77, v41\n" \
  "v_mul_f32 v78, v78, v38\nv_mul_f32 v80, v80, v39\nv_mul_f32 v79, v79, v40\n" \
  "v_mul_f32 v81, v81, v41\nv_mul_f32 v82, v82, v38\nv_mul_f32 v84, v84, v39\n" \
  "v_mul_f32 v83, v83, v40\nv_mul_f32 v85, v85, v41\nv_cvt_pk_bf16_f32 v86, v6, v7\n" \
  "v_cvt_pk_bf16_f32 v87, v8, v9\nv_fmac_f32_dpp v62, v126, v14 row_newbcast:0 row_mask:0xf bank_mask:0xf\nv_fmac_f32_dpp v64, v126, v15 row_newbcast:1 row_mask:0xf bank_mask:0xf\n" \
  "v_fmac_f32_dpp v63, v126, v16 row_newbcast:2 row_mask:0xf bank_mask:0xf\nv_fmac_f32_dpp v65, v126, v17 row_newbcast:3 row_mask:0xf bank_mask:0xf\nv_fmac_f32_dpp v66, v126, v14 row_newbcast:4 row_mask:0xf bank_mask:0xf\n" \
  "v_fmac_f32_dpp v68, v126, v15 row_newbcast:5 row_mask:0xf bank_mask:0xf\nv_fmac_f32_dpp v67, v126, v16 row_newbcast:6 row_mask:0xf bank_mask:0xf\nv_fmac_f32_dpp v69, v126, v17 row_newbcast:7 row_mask:0xf bank_mask:0xf\n" \
  "v_fmac_f32_dpp v70, v126, v14 row_newbcast:8 row_mask:0xf bank_mask:0xf\nv_fmac_f32_dpp v72, v126, v15 row_newbcast:9 row_mask:0xf bank_mask:0xf\nv_fmac_f32_dpp v71, v126, v16 row_newbcast:10 row_mask:0xf bank_mask:0xf\n" \
  "v_fmac_f32_dpp v73, v126, v17 row_newbcast:11 row_mask:0xf bank_mask:0xf\nv_add_u32 v128, s49, v134\nds_read_b128 v[102:105], v128\n" \
  "ds_read_b128 v[106:109], v128 offset:1024\nv_cvt_pk_bf16_f32 v88, v34, v35\nv_cvt_pk_bf16_f32 v89, v36, v37\n" \
  "s_waitcnt lgkmcnt(1)\ns_nop 0\nv_mfma_f32_16x16x32_bf16 v[208:211], v[86:89], v[102:105], v[208:211]\n" \
  "v_cvt_pk_bf16_f32 v90, v62, v64\nv_cvt_pk_bf16_f32 v91, v63, v65\nv_fmac_f32_dpp v74, v127, v42 row_newbcast:0 row_mask:0xf bank_mask:0xf\n" \
  "s_waitcnt lgkmcnt(0)\nv_mfma_f32_16x16x32_bf16 v[224:227], v[86:89], v[106:109], v[224:227]\nv_fmac_f32_dpp v76, v127, v43 row_newbcast:1 row_mask:0xf bank_mask:0xf\n" \
  "v_fmac_f32_dpp v75, v127, v44 row_newbcast:2 row_mask:0xf bank_mask:0xf\nv_fmac_f32_dpp v77, v127, v45 row_newbcast:3 row_mask:0xf bank_mask:0xf\nv_cvt_pk_bf16_f32 v92, v74, v76\n" \
  "v_cvt_pk_bf16_f32 v93, v75, v77\ns_nop 1\nv_mfma_f32_16x16x32_bf16 v[212:215], v[90:93], v[102:105], v[212:215]\n" \
  "v_cvt_pk_bf16_f32 v94, v66, v68\nv_mfma_f32_16x16x32_bf16 v[228:231], v[90:93], v[106:109], v[228:231]\nv_cvt_pk_bf16_f32 v95, v67, v69\n" \
  "v_fmac_f32_dpp v78, v127, v42 row_newbcast:4 row_mask:0xf bank_mask:0xf\nv_fmac_f32_dpp v80, v127, v43 row_newbcast:5 row_mask:0xf bank_mask:0xf\nv_fmac_f32_dpp v79, v127, v44 row_newbcast:6 row_mask:0xf bank_mask:0xf\n" \
  "v_fmac_f32_dpp v81, v127, v45 row_newbcast:7 row_mask:0xf bank_mask:0xf\nv_cvt_pk_bf16_f32 v96, v78, v80\nv_cvt_pk_bf16_f32 v97, v79, v81\n" \
  "s_nop 1\nv_mfma_f32_16x16x32_bf16 v[216:219], v[94:97], v[102:105], v[216:219]\nv_mfma_f32_16x16x32_bf16 v[232:235], v[94:97], v[106:109], v[232:235]\n" \
  "v_cvt_pk_bf16_f32 v98, v70, v72\nv_cvt_pk_bf16_f32 v99, v71, v73\nv_fmac_f32_dpp v82, v127, v42 row_newbcast:8 row_mask:0xf bank_mask:0xf\n" \
  "v_fmac_f32_dpp v84, v127, v43 row_newbcast:9 row_mask:0xf bank_mask:0xf\nv_fmac_f32_dpp v83, v127, v44 row_newbcast:10 row_mask:0xf bank_mask:0xf\nv_fmac_f32_dpp v85, v127, v45 row_newbcast:11 row_mask:0xf bank_mask:0xf\n" \
  "v_cvt_pk_bf16_f32 v100, v82, v84\nv_cvt_pk_bf16_f32 v101, v83, v85\ns_nop 1\n" \
  "v_mfma_f32_16x16x32_bf16 v[220:223], v[98:101], v[102:105], v[220:223]\nv_mfma_f32_16x16x32_bf16 v[236:239], v[98:101], v[106:109], v[236:239]\ns_nop 7\n" \
  "s_branch .Lgen%=\n.Lfl%=:\ns_cmp_gt_u32 s40, 2\n" \
  "s_cbranch_scc1 .Lf2%=\ns_waitcnt vmcnt(0)\ns_barrier\n" \
  ".Lf2%=:\ns_add_u32 s52, s41, 1\ns_cmp_lt_u32 s52, %1\n" \
  "s_cbranch_scc1 .Lslow%=\ns_lshr_b64 s[54:55], s[54:55], 8\ns_add_u32 %1, %1, 1\n" \
  "s_mov_b32 s42, 0\ns_waitcnt lgkmcnt(0)\ns_bitcmp0_b32 %1, 0\n" \
  "s_cbranch_scc1 .Lodd%=\nds_read_b64_tr_b16 v[114:115], v139\nds_read_b64_tr_b16 v[116:117], v139 offset:256\n" \
  "ds_read_b64_tr_b16 v[118:119], v139 offset:512\nds_read_b64_tr_b16 v[120:121], v139 offset:768\ns_waitcnt lgkmcnt(3)\n" \
  "v_mfma_f32_16x16x16_bf16 v[208:211], v[114:115], v[150:151], v[208:211]\ns_waitcnt lgkmcnt(2)\nv_mfma_f32_16x16x16_bf16 v[212:215], v[116:117], v[150:151], v[212:215]\n" \
  "s_waitcnt lgkmcnt(1)\nv_mfma_f32_16x16x16_bf16 v[216:219], v[118:119], v[150:151], v[216:219]\ns_waitcnt lgkmcnt(0)\n" \
  "v_mfma_f32_16x16x16_bf16 v[220:223], v[120:121], v[150:151], v[220:223]\ns_nop 1\nv_cvt_pk_bf16_f32 v102, v208, v209\n" \
  "v_cvt_pk_bf16_f32 v103, v210, v211\nds_write_b64 v138, v[102:103] offset:0\nv_mov_b64 v[208:209], 0\n" \
  "v_mov_b64 v[210:211], 0\nv_cvt_pk_bf16_f32 v104, v212, v213\nv_cvt_pk_bf16_f32 v105, v214, v215\n" \
  "ds_write_b64 v138, v[104:105] offset:256\nv_mov_b64 v[212:213], 0\nv_mov_b64 v[214:215], 0\n" \
  "v_cvt_pk_bf16_f32 v106, v216, v217\nv_cvt_pk_bf16_f32 v107, v218, v219\nds_write_b64 v138, v[106:107] offset:512\n" \
  "v_mov_b64 v[216:217], 0\nv_mov_b64 v[218:219], 0\nv_cvt_pk_bf16_f32 v108, v220, v221\n" \
  "v_cvt_pk_bf16_f32 v109, v222, v223\nds_write_b64 v138, v[108:109] offset:768\nv_mov_b64 v[220:221], 0\n" \
  "v_mov_b64 v[222:223], 0\ns_branch .Lgen%=\n.Lodd%=:\n" \
  "ds_read_b64_tr_b16 v[114:115], v139 offset:16640\nds_read_b64_tr_b16 v[116:117], v139 offset:16896\nds_read_b64_tr_b16 v[118:119], v139 offset:17152\n" \
  "ds_read_b64_tr_b16 v[120:121], v139 offset:17408\ns_waitcnt lgkmcnt(3)\nv_mfma_f32_16x16x16_bf16 v[224:227], v[114:115], v[150:151], v[224:227]\n" \
  "s_waitcnt lgkmcnt(2)\nv_mfma_f32_16x16x16_bf16 v[228:231], v[116:117], v[150:151], v[228:231]\ns_waitcnt lgkmcnt(1)\n" \
  "v_mfma_f32_16x16x16_bf16 v[232:235], v[118:119], v[150:151], v[232:235]\ns_waitcnt lgkmcnt(0)\nv_mfma_f32_16x16x16_bf16 v[236:239], v[120:121], v[150:151], v[236:239]\n" \
  "s_nop 1\nv_cvt_pk_bf16_f32 v102, v224, v225\nv_cvt_pk_bf16_f32 v103, v226, v227\n" \
  "ds_write_b64 v138, v[102:103] offset:16640\nv_mov_b64 v[224:225], 0\nv_mov_b64 v[226:227], 0\n" \
  "v_cvt_pk_bf16_f32 v104, v228, v229\nv_cvt_pk_bf16_f32 v105, v230, v231\nds_write_b64 v138, v[104:105] offset:16896\n" \
  "v_mov_b64 v[228:229], 0\nv_mov_b64 v[230:231], 0\nv_cvt_pk_bf16_f32 v106, v232, v233\n" \
  "v_cvt_pk_bf16_f32 v107, v234, v235\nds_write_b64 v138, v[106:107] offset:17152\nv_mov_b64 v[232:233], 0\n" \
  "v_mov_b64 v[234:235], 0\nv_cvt_pk_bf16_f32 v108, v236, v237\nv_cvt_pk_bf16_f32 v109, v238, v239\n" \
  "ds_write_b64 v138, v[108:109] offset:17408\nv_mov_b64 v[236:237], 0\nv_mov_b64 v[238:239], 0\n" \
  "s_branch .Lgen%=\n.Lslow%=:\ns_mov_b32 s41, %0\n" \
  "s_lshl_b32 s51, %0, 14\ns_add_u32 s51, s51, %7\ns_add_u32 s53, s51, 0x10000\n" \
  "s_mov_b32 s42, 0\ns_waitcnt lgkmcnt(0)\ns_barrier\n" \
  "s_cmp_eq_u32 %0, %1\ns_cbranch_scc1 .Lgen%=\ns_cmp_ge_u32 %0, 2\n" \
  "s_cbranch_scc1 .Ls4%=\ns_waitcnt vmcnt(0)\n.Ls4%=:\n" \
  "s_waitcnt vmcnt(4)\ns_add_u32 %0, %0, 1\ns_bitcmp0_b32 %0, 0\n" \
  "s_cbranch_scc1 .Ls1%=\nds_read_b128 v[114:117], v146\nds_read_b128 v[118:121], v146 offset:1040\n" \
  "s_waitcnt lgkmcnt(1)\nbuffer_store_dwordx4 v[114:117], v134, %5, s51 offen nt\nds_write_b128 v146, v[240:243] offset:0\n" \
  "buffer_load_dwordx4 v[240:243], v134, %4, s53 offen\ns_waitcnt lgkmcnt(1)\nbuffer_store_dwordx4 v[118:121], v134, %5, s51 offen offset:1024 nt\n" \
  "ds_write_b128 v146, v[244:247] offset:1040\nbuffer_load_dwordx4 v[244:247], v134, %4, s53 offen offset:1024\ns_branch .Lgen%=\n" \
  ".Ls1%=:\nds_read_b128 v[114:117], v146 offset:16640\nds_read_b128 v[118:121], v146 offset:17680\n" \
  "s_waitcnt lgkmcnt(1)\nbuffer_store_dwordx4 v[114:117], v134, %5, s51 offen nt\nds_write_b128 v146, v[248:251] offset:16640\n" \
  "buffer_load_dwordx4 v[248:251], v134, %4, s53 offen\ns_waitcnt lgkmcnt(1)\nbuffer_store_dwordx4 v[118:121], v134, %5, s51 offen offset:1024 nt\n" \
  "ds_write_b128 v146, v[252:255] offset:17680\nbuffer_load_dwordx4 v[252:255], v134, %4, s53 offen offset:1024\ns_branch .Lgen%=\n" \
  ".Lend%=:\n" \
  ""
#define SETUP_ASM \
  "v_mov_b32 v132, %0\nv_mov_b32 v133, %1\nv_mov_b32 v134, %2\nv_mov_b32 v135, %3\n" \
  "v_mov_b32 v136, %4\nv_mov_b32 v137, %5\nv_mov_b32 v138, %6\nv_mov_b32 v139, %7\n" \
  "v_mov_b32 v140, %8\nv_mov_b32 v141, %9\nv_mov_b32 v142, %10\nv_mov_b32 v143, %11\n" \
  "v_mov_b32 v144, %12\nv_mov_b32 v145, %13\nv_mov_b32 v146, %14\nv_mov_b64 v[148:149], %15\n" \
  "v_mov_b64 v[150:151], %16\nv_mov_b32 v147, 0xffff0000\nv_mov_b32 v153, 0\nv_mov_b64 v[208:209], 0\n" \
  "v_mov_b64 v[210:211], 0\nv_mov_b64 v[212:213], 0\nv_mov_b64 v[214:215], 0\nv_mov_b64 v[216:217], 0\n" \
  "v_mov_b64 v[218:219], 0\nv_mov_b64 v[220:221], 0\nv_mov_b64 v[222:223], 0\nv_mov_b64 v[224:225], 0\n" \
  "v_mov_b64 v[226:227], 0\nv_mov_b64 v[228:229], 0\nv_mov_b64 v[230:231], 0\nv_mov_b64 v[232:233], 0\n" \
  "v_mov_b64 v[234:235], 0\nv_mov_b64 v[236:237], 0\nv_mov_b64 v[238:239], 0\n" \
  ""
#define LOOP_CLOB \
  "v6", "v7", "v8", "v9", "v10", "v11", "v12", "v13", "v14", "v15", "v16", "v17", "v18", "v19", "v20", "v21", \
  "v22", "v23", "v24", "v25", "v26", "v27", "v28", "v29", "v30", "v31", "v32", "v33", "v34", "v35", "v36", "v37", \
  "v38", "v39", "v40", "v41", "v42", "v43", "v44", "v45", "v46", "v47", "v48", "v49", "v50", "v51", "v52", "v53", \
  "v54", "v55", "v56", "v57", "v58", "v59", "v60", "v61", "v62", "v63", "v64", "v65", "v66", "v67", "v68", "v69", \
  "v70", "v71", "v72", "v73", "v74", "v75", "v76", "v77", "v78", "v79", "v80", "v81", "v82", "v83", "v84", "v85", \
  "v86", "v87", "v88", "v89", "v90", "v91", "v92", "v93", "v94", "v95", "v96", "v97", "v98", "v99", "v100", "v101", \
  "v102", "v103", "v104", "v105", "v106", "v107", "v108", "v109", "v110", "v111", "v112", "v113", "v114", "v115", "v116", "v117", \
  "v118", "v119", "v120", "v121", "v122", "v123", "v124", "v125", "v126", "v127", "s40", "s41", "s42", "s43", "s44", "s45", \
  "s46", "s47", "s48", "s49", "s50", "s51", "s52", "s53", "s54", "s55", "s56", "s57", "s58", "s59", "s60", "s61", \
  "s62", "s63", "s64", "s65", "s66", "s67", "s68", "s69"

constexpr int CAPL = CAPT * 32;
constexpr int WIN = CAPL - 96;
constexpr int L_GD = L_S;
constexpr int L_MAIN_TOTAL = L_MISC + 128;
static_assert(L_MAIN_TOTAL <= 163840, "lds");

DI float bflo(u32 x) { return __uint_as_float(x << 16); }
DI float bfhi(u32 x) { return __uint_as_float(x & 0xffff0000u); }
DI u32 pk2(float a, float b) { f32x2 f = {a, b}; return __builtin_bit_cast(u32, __builtin_convertvector(f, bf16x2)); }
DI float rb(float a) { return bflo(pk2(a, a)); }
DI int cell_col(int b) { return ((b & 7) << 4) | (b >> 3); }

DI float red2pi(float x) {
  const float chi = 0.15915494309189535f;
  const float clo = (float)(0.15915494309189533577 - (double)chi);
  const float k = __builtin_rintf(x * chi);
  return __builtin_fmaf(x, clo, __builtin_fmaf(x, chi, -k));
}
DI void unpack_pos(u32 lo, u32 hi, u32 n, float& x, float& y, float& z) {
  const u32 sh = (n << 4) & 31u;
  const u32 xy = __builtin_amdgcn_alignbit(hi, lo, sh);
  x = bflo(xy); y = bfhi(xy); z = bflo(hi >> sh);
}
DI float edge_sq(u32x2 ps, u32 s, u32x2 pd, u32 d, float& rx, float& ry, float& rz) {
  float sx, sy, sz, dx, dy, dz;
  unpack_pos(ps[0], ps[1], s, sx, sy, sz);
  unpack_pos(pd[0], pd[1], d, dx, dy, dz);
  const u32 a = pk2(sx - dx, sy - dy);
  rx = bflo(a); ry = bfhi(a); rz = rb(sz - dz);
  const u32 b = pk2(rx * rx, ry * ry);
  return (bflo(b) + bfhi(b)) + rb(rz * rz);
}

DI u32x4 make_srd(const void* p, u32 bytes) {
  const u64 a = (u64)p;
  return u32x4{(u32)a, (u32)(a >> 32) & 0xffffu, bytes, 0x00020000u};
}
DI u32x4 pack_unit(const u16* w2, const u16* wr, int u) {
  const int l = u & 63, slot = (u >> 6) % WUNITS, ww = (u >> 6) / WUNITS;
  const int col = l & 15, q = l >> 4;
  u16 t[8];
  if (slot < 12) {
    const int grp = slot >> 2, s = slot & 3;
#pragma unroll
    for (int j = 0; j < 8; ++j) t[j] = w2[(32 * s + 8 * q + j) * 384 + grp * 128 + 16 * ww + col];
  } else {
#pragma unroll
    for (int j = 0; j < 8; ++j) {
      const int grp = (slot - 12) * 2 + (j >> 2);
      t[j] = grp < 3 ? wr[(4 * q + (j & 3)) * 384 + grp * 128 + 16 * ww + col] : (u16)0;
    }
  }
  return u32x4{t[0] | ((u32)t[1] << 16), t[2] | ((u32)t[3] << 16), t[4] | ((u32)t[5] << 16), t[6] | ((u32)t[7] << 16)};
}
DI u32 silu2(float a, float b) {
  const u32 xp = pk2(a, b);
  const f32x2 x = {bflo(xp), bfhi(xp)}, t = x * -1.4426950408889634f;
  const f32x2 d = f32x2{__builtin_amdgcn_exp2f(t[0]), __builtin_amdgcn_exp2f(t[1])} + 1.f;
  const f32x2 y = x * f32x2{__builtin_amdgcn_rcpf(d[0]), __builtin_amdgcn_rcpf(d[1])};
  return pk2(y[0], y[1]);
}
struct PrepArgs {
  const u16* sv; const u16* pos; const int* src; const int* dst; const u16* w1; const u16* w2; const u16* wr;
  u32* cells; u64* ovf; u32* ovfcnt; u16* h; u32x4* wpack; char* ws;
  int N, E, epb, G, npg; u32 npg_magic;
};

#define TS(i) do {} while (0)
#define TSOUT(k) do {} while (0)
template <bool POS_LDS>
__global__ __launch_bounds__(1024) __attribute__((amdgpu_num_vgpr(60))) void k_prep(PrepArgs a) {
  extern __shared__ __attribute__((aligned(16))) char smem[];
  const int tid = threadIdx.x, lane = tid & 63, w = __builtin_amdgcn_readfirstlane(tid >> 6);
  const int b = blockIdx.x;
  TS(0);
  if (b < NBIN) {
    constexpr int EPP = 4096;
    const int posb = POS_LDS ? ((a.N * 6 + 15) & ~15) : 0;
    u32* cnt = (u32*)(smem + posb);
    const int e0 = b * a.epb, e1 = min(a.E, e0 + a.epb);
    const u32x4 rs = make_srd(a.src, a.E * 4), rd = make_srd(a.dst, a.E * 4);
    const u32 ldsb = (u32)(size_t)(__attribute__((address_space(3))) char*)smem;
    const auto rc = __builtin_amdgcn_make_buffer_rsrc((void*)(a.cells + cell_col(b) * CELLW), 0, 0x7fffffff, 0x00020000);
    {
      bool staged = false;
      for (int eb = e0; eb < e1 || !staged; eb += EPP) {
        const int e = eb + tid * 4, off = e < e1 ? e * 4 : 0x7ffffff0;
        u32x4 s4, d4;
        asm volatile("buffer_load_dwordx4 %0, %2, %3, 0 offen\n\tbuffer_load_dwordx4 %1, %2, %4, 0 offen" : "=&v"(s4), "=&v"(d4) : "v"(off), "s"(rs), "s"(rd) : "memory");
        if (!staged) {
          for (int i = tid; i <= a.G; i += 1024) cnt[i] = 0;
          if (POS_LDS) {
            const int np = (posb + 1023) >> 10, rot = ((b >> 3) * np) >> 4;
            const int hi = (np * 6) >> 4;
            for (int i = w >= 12 ? w - 12 : hi + w; i < (w >= 12 ? hi : np); i += (w >= 12 ? 4 : 12)) {
              int j = i + rot; if (j >= np) j -= np;
              const u32 o = min(j * 1024 + lane * 16, posb - 16);
              if (j * 1024 + lane * 16 < posb)
                asm volatile("s_mov_b32 m0, %2\n\ts_nop 0\n\tglobal_load_lds_dwordx4 %0, %1" :: "v"(o), "s"(a.pos), "s"(ldsb + j * 1024) : "memory");
            }
          }
        }
        if (!staged && POS_LDS && posb >= 144 * 1024) asm volatile("s_waitcnt vmcnt(4)" ::: "memory");
        else asm volatile("s_waitcnt vmcnt(0)" ::: "memory");
        asm volatile("" : "+v"(s4), "+v"(d4));
        TS(6);
        u32 os[4], od[4], ss[4], sd[4], rec[4], ca[4], co[4]; float lim[4];
#pragma unroll
        for (int i = 0; i < 4; ++i) {
          os[i] = (s4[i] + (s4[i] >> 1)) << 2; od[i] = (d4[i] + (d4[i] >> 1)) << 2;
          ss[i] = 0x01000c0cu + (s4[i] & 1u) * 0x02020000u; sd[i] = 0x01000c0cu + (d4[i] & 1u) * 0x02020000u;
          const u32 g = __umulhi(d4[i], a.npg_magic);
          rec[i] = s4[i] | ((d4[i] - g * a.npg) << 24);
          ca[i] = posb + g * 4; co[i] = g * (NCELL * CELLW * 4) + 4;
          lim[i] = e + i < e1 ? 24.8125f : -1.f;
          asm volatile("" : "+v"(os[i]), "+v"(od[i]), "+v"(ss[i]), "+v"(sd[i]), "+v"(rec[i]), "+v"(ca[i]), "+v"(co[i]), "+v"(lim[i]));
        }
        if (!staged) { asm volatile("s_waitcnt vmcnt(0) lgkmcnt(0)\n\ts_barrier" ::: "memory"); staged = true; TS(1); }
        if (eb + w * 256 >= e1) continue;
        u32x2 ps[4], pd[4];
#pragma unroll
        for (int i = 0; i < 4; ++i) {
          const char* base = POS_LDS ? (const char*)smem : (const char*)a.pos;
          const u32* p1 = (const u32*)(base + os[i]); const u32* p2 = (const u32*)(base + od[i]);
          ps[i] = u32x2{p1[0], p1[1]}; pd[i] = u32x2{p2[0], p2[1]};
        }
        float rx[4], ry[4], rz[4];
#pragma unroll
        for (int i = 0; i < 4; ++i) {
          rx[i] = __uint_as_float(__builtin_amdgcn_perm(ps[i][1], ps[i][0], ss[i])) - __uint_as_float(__builtin_amdgcn_perm(pd[i][1], pd[i][0], sd[i]));
          ry[i] = __uint_as_float(__builtin_amdgcn_perm(ps[i][1], ps[i][0], ss[i] + 0x02020000u)) - __uint_as_float(__builtin_amdgcn_perm(pd[i][1], pd[i][0], sd[i] + 0x02020000u));
          rz[i] = __uint_as_float(__builtin_amdgcn_perm(ps[i][1], ps[i][0], ss[i] + 0x04040000u)) - __uint_as_float(__builtin_amdgcn_perm(pd[i][1], pd[i][0], sd[i] + 0x04040000u));
        }
        u32 r[4];
#pragma unroll
        for (int i = 0; i < 4; i += 2) {
          const u32 x = pk2(rx[i], rx[i + 1]), y = pk2(ry[i], ry[i + 1]), z = pk2(rz[i], rz[i + 1]);
          const u32 x2 = pk2(bflo(x) * bflo(x), bfhi(x) * bfhi(x)), y2 = pk2(bflo(y) * bflo(y), bfhi(y) * bfhi(y)), z2 = pk2(bflo(z) * bflo(z), bfhi(z) * bfhi(z));
          r[i] = r[i + 1] = 0x10000000u;
          if ((bflo(x2) + bflo(y2)) + bflo(z2) <= lim[i]) r[i] = atomicAdd((u32*)(smem + ca[i]), 1u);
          if ((bfhi(x2) + bfhi(y2)) + bfhi(z2) <= lim[i + 1]) r[i + 1] = atomicAdd((u32*)(smem + ca[i + 1]), 1u);
        }
        TS(7);
        bool over = false;
#pragma unroll
        for (int i = 0; i < 4; ++i) {
          if (r[i] < CELLCAP) __builtin_amdgcn_raw_buffer_store_b32(rec[i], rc, co[i] + r[i] * 4, 0, 0);
          over = over || (r[i] - CELLCAP < 0x10000000u - CELLCAP);
        }
        if (over) {
#pragma unroll 1
          for (int i = 0; i < 4; ++i) {
            const u32 ri = i == 0 ? r[0] : i == 1 ? r[1] : i == 2 ? r[2] : r[3], rci = i == 0 ? rec[0] : i == 1 ? rec[1] : i == 2 ? rec[2] : rec[3];
            const u32 cai = i == 0 ? ca[0] : i == 1 ? ca[1] : i == 2 ? ca[2] : ca[3];
            if (ri - CELLCAP < 0x10000000u - CELLCAP) { const u32 o = atomicAdd(&cnt[a.G], 1u); a.ovf[(size_t)b * a.epb + o] = ((u64)((cai - posb) >> 2) << 32) | rci; }
          }
        }
      }
    }
    asm volatile("s_waitcnt lgkmcnt(0)\n\ts_barrier" ::: "memory");
    TS(2);
    for (int i = tid; i < a.G; i += 1024) __builtin_amdgcn_raw_buffer_store_b32(cnt[i], rc, i * (NCELL * CELLW * 4), 0, 0);
    if (tid == 0) a.ovfcnt[b] = cnt[a.G];
    TS(3); TSOUT(0);
  } else {
    const int gb = b - NBIN, ngemm = gridDim.x - NBIN;
    char* w_img = smem; char* s_img = smem + 32768;
    const u32 ldsb = (u32)(size_t)(__attribute__((address_space(3))) char*)smem;
    const int jump = (ngemm - 1) * a.npg;
    auto node = [&](int row) { return gb * a.npg + row + (row >= a.npg ? jump : 0); };
    const int nrow = 2 * a.npg;
#pragma unroll
    for (int k = 0; k < 2; ++k) {
      const int i = w * 2 + k, row = 4 * i + (lane >> 4);
      asm volatile("s_mov_b32 m0, %2\n\ts_nop 0\n\tglobal_load_lds_dwordx4 %0, %1"
                   :: "v"(row * 256 + 16 * ((lane & 15) ^ ((row & 3) << 2))), "s"(a.w1), "s"(ldsb + i * 1024) : "memory");
    }
    for (int i = w; i * 4 < nrow; i += 16) {
      const int row = 4 * i + (lane >> 4);
      const u32 n = min(node(row), a.N - 1);
      asm volatile("s_mov_b32 m0, %2\n\ts_nop 0\n\tglobal_load_lds_dwordx4 %0, %1"
                   :: "v"(n * 1024u + 16 * ((lane & 15) ^ (row & 15))), "s"(a.sv), "s"(ldsb + 32768 + i * 1024) : "memory");
    }
    if (w == 15) {
      for (int u = gb * 64 + lane; u < 8 * WUNITS * 64; u += ngemm * 64) a.wpack[u] = pack_unit(a.w2, a.wr, u);
    }
    asm volatile("s_waitcnt vmcnt(0)\n\ts_barrier" ::: "memory");
    TS(4);
    const int rg = w >> 2, ct = w & 3;
    const int r16 = lane & 15, q4 = lane >> 4, qq = (lane >> 2) & 3, p = lane & 3;
    const auto rh = __builtin_amdgcn_make_buffer_rsrc((void*)a.h, 0, a.N * 256, 0x00020000);
    bf16x8 aw[2][4];
#pragma unroll
    for (int ks = 0; ks < 4; ++ks)
#pragma unroll
      for (int m = 0; m < 2; ++m) {
        const char* ap = w_img + (32 * ks + 8 * q4 + qq) * 256 + ((64 * ct + 16 * p + 8 * m) ^ (qq << 6));
        const s16x4 lo = __builtin_amdgcn_ds_read_tr16_b64_v4i16((lds_s16x4_p)ap);
        const s16x4 hi = __builtin_amdgcn_ds_read_tr16_b64_v4i16((lds_s16x4_p)(ap + 1024));
        aw[m][ks] = __builtin_bit_cast(bf16x8, __builtin_shufflevector(lo, hi, 0, 1, 2, 3, 4, 5, 6, 7));
      }
#pragma unroll 1
    for (int t = rg; t * 16 < nrow; t += 4) {
      bf16x8 bs[4];
#pragma unroll
      for (int ks = 0; ks < 4; ++ks) bs[ks] = *(const bf16x8*)(s_img + (t * 16 + r16) * 256 + 16 * ((4 * ks + q4) ^ r16));
      f32x4 acc[2] = {f32x4{0.f, 0.f, 0.f, 0.f}, f32x4{0.f, 0.f, 0.f, 0.f}};
#pragma unroll
      for (int ks = 0; ks < 4; ++ks)
#pragma unroll
        for (int m = 0; m < 2; ++m) acc[m] = __builtin_amdgcn_mfma_f32_16x16x32_bf16(aw[m][ks], bs[ks], acc[m], 0, 0, 0);
      u32x4 o;
#pragma unroll
      for (int i = 0; i < 4; ++i) o[i] = silu2(acc[i >> 1][2 * (i & 1)], acc[i >> 1][2 * (i & 1) + 1]);
      const int row = t * 16 + r16;
      __builtin_amdgcn_raw_buffer_store_b128(o, rh, row < nrow ? (u32)node(row) * 256u + ct * 64 + q4 * 16 : 0x7ffffff0u, 0, 2);
    }
    TS(5); TSOUT(0);
  }
}

struct MainArgs {
  const u16* sv; const u16* pos; const u16* freqs; u32* cells; u64* ovf; u32* ovfcnt;
  u16* h; u32x4* wpack; u16* out; char* ws;
  const int* src; const int* dst; const u16* w1; const u16* w2; const u16* wr; u32* sync;
  int N, epb, npg, E, G; u32 npg_magic; int nbin, rpb;
};

DI void lds_barrier() { asm volatile("s_waitcnt lgkmcnt(0)\n\ts_barrier" ::: "memory"); }
#define WVM(n) asm volatile("s_waitcnt vmcnt(" #n ")" ::: "memory")
DI u32 wave_incl_scan(u32 x) {
  x += __builtin_amdgcn_update_dpp(0, x, 0x111, 0xf, 0xf, true);
  x += __builtin_amdgcn_update_dpp(0, x, 0x112, 0xf, 0xf, true);
  x += __builtin_amdgcn_update_dpp(0, x, 0x114, 0xf, 0xf, true);
  x += __builtin_amdgcn_update_dpp(0, x, 0x118, 0xf, 0xf, true);
  x += __builtin_amdgcn_update_dpp(0, x, 0x142, 0xa, 0xf, false);
  x += __builtin_amdgcn_update_dpp(0, x, 0x143, 0xc, 0xf, false);
  return x;
}

#define LDW2(p) asm volatile("global_load_dwordx4 v[202:205], %0, %1 sc1\n\tglobal_load_dwordx2 v[206:207], %0, %1 offset:1024 sc1" :: "v"(lane * 16), "s"(p + 768) : "memory", "v255")
#define LDW4(r, p) asm volatile("global_load_dwordx4 v[" #r ":" #r "+3], %0, %1 sc1\n\tglobal_load_dwordx4 v[" #r "+4:" #r "+7], %0, %1 offset:1024 sc1\n\t" \
                 "global_load_dwordx4 v[" #r "+8:" #r "+11], %0, %1 offset:2048 sc1\n\tglobal_load_dwordx4 v[" #r "+12:" #r "+15], %0, %1 offset:3072 sc1" \
                 :: "v"(lane * 16), "s"(p) : "memory")
DI void load_weights(const u32x4* wp, int lane) { LDW2(wp); LDW4(154, wp); LDW4(170, wp + 256); LDW4(186, wp + 512); }

constexpr int FS = 8, SY_W = 64, SY_H = SY_W + 128 * FS, NPACK = 8 * WUNITS, SPIN_MAX = 1 << 20;
constexpr u32 TAG_MASK = 0x80ff8000u, REC_NONE = 0x7f000000u, REC_MORE = 0x7e000000u;
DI u32x4 cell_tags(u32 t) {
  return u32x4{(t & 511u) << 15, ((t + (t >> 9) + 101u) & 511u) << 15, ((t + (t >> 18) + 202u) & 511u) << 15, ((t + (t >> 27) + 303u) & 511u) << 15};
}
DI void flags_request(const u32* f, int n, int lane) {
  asm volatile("global_load_dword v128, %0, %3 sc1\n\tglobal_load_dword v129, %1, %3 sc1\n\tglobal_load_dword v130, %2, %3 sc1"
               :: "v"(min(lane, n - 1) * FS * 4), "v"(min(lane + 64, n - 1) * FS * 4), "v"(min(lane + 128, n - 1) * FS * 4), "s"(f) : "memory");
}
DI bool flags_ok(u32 t) {
  u32 x;
  asm volatile("v_xor_b32 v128, %1, v128\n\tv_xor_b32 v129, %1, v129\n\tv_xor_b32 v130, %1, v130\n\tv_or3_b32 %0, v128, v129, v130" : "=v"(x) : "s"(t) : "memory");
  return __builtin_amdgcn_ballot_w64(x != 0) == 0;
}
DI void flags_wait(const u32* f, int n, u32 t, int lane) {
#pragma unroll 1
  for (int s = 0; s < SPIN_MAX; ++s) {
    flags_request(f, n, lane);
    WVM(0);
    if (flags_ok(t)) break;
    __builtin_amdgcn_s_sleep(1);
  }
}
DI void flag_set(u32* f, u32 t) { __hip_atomic_store(f, t, __ATOMIC_RELAXED, __HIP_MEMORY_SCOPE_AGENT); }

typedef const __attribute__((address_space(4))) MainArgs KArgs;
DI void first_phase(KArgs& a, char* smem, u32& tag) {
  const int tid = threadIdx.x, lane = tid & 63, w = __builtin_amdgcn_readfirstlane(tid >> 6), b = blockIdx.x;
  const u32 ldsb = (u32)(size_t)(__attribute__((address_space(3))) char*)smem;
  TS(0);
  auto touch = [&](int k0, int n) {
    if (w == 7)
      for (int k = k0; k < k0 + n; ++k)
        asm volatile("global_load_dword v131, %0, %1" :: "v"(min(b * a.npg * 8 + k * 64 + lane, a.N * 8 - 1) * 128u), "s"(a.sv) : "memory");
  };
  if (b < a.nbin) {
    const int posb = (a.N * 6 + 15) & ~15;
    u32* cnt = (u32*)(smem + posb);
    const int e0 = b * a.epb, e1 = min(a.E, e0 + a.epb);
    const u32x4 rs = make_srd(a.src, a.E * 4), rd = make_srd(a.dst, a.E * 4);
    const int e = e0 + tid * 4;
    u32x4 s4[2], d4[2];
    asm volatile("buffer_load_dwordx4 %0, %4, %6, 0 offen\n\tbuffer_load_dwordx4 %1, %4, %7, 0 offen\n\t"
                 "buffer_load_dwordx4 %2, %5, %6, 0 offen\n\tbuffer_load_dwordx4 %3, %5, %7, 0 offen"
                 : "=&v"(s4[0]), "=&v"(d4[0]), "=&v"(s4[1]), "=&v"(d4[1])
                 : "v"(e < e1 ? e * 4 : 0x7ffffff0), "v"(e + 2048 < e1 ? e * 4 + 8192 : 0x7ffffff0), "s"(rs), "s"(rd) : "memory");
    if (tid <= a.G) cnt[tid] = 0;
    const int np = (posb + 1023) >> 10, rot = ((b >> 3) * np) >> 4;
    for (int i = w; i < np; i += 8) {
      int j = i + rot; if (j >= np) j -= np;
      const u32 o = min(j * 1024 + lane * 16, posb - 16);
      if (j * 1024 + lane * 16 < posb)
        asm volatile("s_mov_b32 m0, %2\n\ts_nop 0\n\tglobal_load_lds_dwordx4 %0, %1" :: "v"(o), "s"(a.pos), "s"(ldsb + j * 1024) : "memory");
    }
    if (np >= 40) WVM(4); else WVM(0);
    asm volatile("" : "+v"(s4[0]), "+v"(d4[0]), "+v"(s4[1]), "+v"(d4[1]));
    TS(6);
    u32 os[8], od[8], ss[8][3], sd[8][3], rec[8], ca[8], r[8];
#pragma unroll
    for (int i = 0; i < 8; ++i) {
      const u32 s = s4[i >> 2][i & 3], d = d4[i >> 2][i & 3];
      os[i] = (s + (s >> 1)) << 2; od[i] = (d + (d >> 1)) << 2;
#pragma unroll
      for (int c = 0; c < 3; ++c) { ss[i][c] = 0x01000c0cu + ((s & 1u) + c) * 0x02020000u; sd[i][c] = 0x01000c0cu + ((d & 1u) + c) * 0x02020000u; }
      if (e + (i >> 2) * 2048 + (i & 3) >= e1) ss[i][0] = 0x0d0d0d0du;
      const u32 g = __umulhi(d, a.npg_magic);
      rec[i] = s | ((d - g * a.npg) << 24);
      ca[i] = g * 4;
      r[i] = 0x10000000u;
      asm volatile("" : "+v"(os[i]), "+v"(od[i]), "+v"(ss[i][0]), "+v"(ss[i][1]), "+v"(ss[i][2]), "+v"(sd[i][0]), "+v"(sd[i][1]), "+v"(sd[i][2]), "+v"(rec[i]), "+v"(ca[i]));
    }
    asm volatile("s_waitcnt vmcnt(0) lgkmcnt(0)\n\tv_readfirstlane_b32 %0, v131\n\ts_barrier" : "=s"(tag) :: "memory");
    tag += 1;
    TS(1);
#pragma unroll
    for (int k = 0; k < 8; k += 4) {
      if (e0 + k * 512 + w * 256 >= e1) continue;
      u32x2 ps[4], pd[4];
#pragma unroll
      for (int i = 0; i < 4; ++i) {
        const u32* p1 = (const u32*)(smem + os[k + i]); const u32* p2 = (const u32*)(smem + od[k + i]);
        ps[i] = u32x2{p1[0], p1[1]}; pd[i] = u32x2{p2[0], p2[1]};
      }
#pragma unroll
      for (int i = 0; i < 4; ++i) {
        float q = 0.f;
#pragma unroll
        for (int c = 0; c < 3; ++c) {
          const float x = __uint_as_float(__builtin_amdgcn_perm(ps[i][1], ps[i][0], ss[k + i][c])) - __uint_as_float(__builtin_amdgcn_perm(pd[i][1], pd[i][0], sd[k + i][c]));
          q = c ? __builtin_fmaf(x, x, q) : x * x;
        }
        if (q <= 24.97f) r[k + i] = atomicAdd((u32*)((char*)cnt + ca[k + i]), 1u);
      }
    }
    flags_request(a.sync + SY_W, NPACK, lane);
    lds_barrier();
    TS(7);
    const u32x4* wp = a.wpack + w * WUNITS * 64;

    const u32 img = posb >= 16640 ? 0 : posb + 2048;
    bool over = false;
#pragma unroll
    for (int i = 0; i < 8; ++i) {
      *(u32*)(smem + img + (r[i] < CELLCAP ? ca[i] * 16 + r[i] * 4 : 16384 + lane * 4)) = rec[i];
      over = over || (r[i] - CELLCAP < 0x10000000u - CELLCAP);
    }
    if (__builtin_amdgcn_ballot_w64(over) != 0) {
#pragma unroll
      for (int i = 0; i < 8; ++i)
        if (r[i] - CELLCAP < 0x10000000u - CELLCAP)
          __hip_atomic_store(a.ovf + (size_t)b * a.epb + atomicAdd(&cnt[a.G], 1u), ((u64)(ca[i] >> 2) << 32) | rec[i], __ATOMIC_RELAXED, __HIP_MEMORY_SCOPE_AGENT);
      WVM(0);
    }
    lds_barrier();
    TS(2);
    const u32 novf = __builtin_amdgcn_readfirstlane(cnt[a.G]);
    if (novf != 0) {
      if (tid == 0) flag_set(a.ovfcnt + b, novf);
      WVM(0); lds_barrier();
    }
    const u32x4 tg = cell_tags(tag);
    const auto rc = __builtin_amdgcn_make_buffer_rsrc((void*)(a.cells + (size_t)b * a.G * CELLW), 0, a.G * CELLW * 4, 0x00020000);
#pragma unroll
    for (int k = 0; k < 2; ++k) {
      const int q = tid + 512 * k;
      u32x4 x = *(const u32x4*)(smem + img + q * 16);
      const int c = (int)cnt[q >> 2] - (q & 3) * 4;
#pragma unroll
      for (int i = 0; i < 4; ++i) x[i] = (i < c ? x[i] : REC_NONE) | tg[i];
      if ((q & 3) == 3) x[3] = (c > 3 ? REC_MORE : REC_NONE) | tg[3];
      __builtin_amdgcn_raw_buffer_store_b128(x, rc, q * 16, 0, 16);
    }
    TS(3);
    WVM(2);
    if (!flags_ok(tag)) flags_wait(a.sync + SY_W, NPACK, tag, lane);
    LDW2(wp); LDW4(154, wp);
    lds_barrier();
  } else {
    const int gb = b - a.nbin, nrow = a.rpb, row0 = gb * nrow;
    char* w_img = smem; char* s_img = smem + 32768;
    const bool packer = w == 7 && gb < NPACK;
    if (w < 7) {
      for (int i = w; i < 32; i += 7) {
        const int row = 4 * i + (lane >> 4);
        asm volatile("s_mov_b32 m0, %2\n\ts_nop 0\n\tglobal_load_lds_dwordx4 %0, %1"
                     :: "v"(row * 256 + 16 * ((lane & 15) ^ ((row & 3) << 2))), "s"(a.w1), "s"(ldsb + i * 1024) : "memory");
      }
#pragma unroll
      for (int k = 0; k < 8; ++k) {
        const int i = 7 * k + w, row = 4 * i + (lane >> 4);
        const u32 n = min(row0 + min(row, nrow - 1), a.N - 1);
        asm volatile("s_mov_b32 m0, %2\n\ts_nop 0\n\tglobal_load_lds_dwordx4 %0, %1"
                     :: "v"(n * 1024u + 16 * ((lane & 15) ^ (row & 15))), "s"(a.sv), "s"(ldsb + 32768 + i * 1024) : "memory");
      }
      WVM(5);
    } else if (packer) {
      const auto rw = __builtin_amdgcn_make_buffer_rsrc((void*)a.wpack, 0, 8 * WUNITS * 1024, 0x00020000);
      __builtin_amdgcn_raw_buffer_store_b128(pack_unit(a.w2, a.wr, gb * 64 + lane), rw, gb * 1024 + lane * 16, 0, 16);
      WVM(1);
    } else WVM(0);
    asm volatile("v_readfirstlane_b32 %0, v131\n\ts_barrier" : "=s"(tag) :: "memory");
    tag += 1;
    TS(4);
    const int rg = w >> 2, ct = w & 3;
    const int r16 = lane & 15, q4 = lane >> 4, qq = (lane >> 2) & 3, p = lane & 3;
    const auto rh = __builtin_amdgcn_make_buffer_rsrc((void*)a.h, 0, a.N * 256, 0x00020000);
    bf16x8 aw[2][4];
#pragma unroll
    for (int ks = 0; ks < 4; ++ks)
#pragma unroll
      for (int m = 0; m < 2; ++m) {
        const char* ap = w_img + (32 * ks + 8 * q4 + qq) * 256 + ((64 * ct + 16 * p + 8 * m) ^ (qq << 6));
        const s16x4 lo = __builtin_amdgcn_ds_read_tr16_b64_v4i16((lds_s16x4_p)ap);
        const s16x4 hi = __builtin_amdgcn_ds_read_tr16_b64_v4i16((lds_s16x4_p)(ap + 1024));
        aw[m][ks] = __builtin_bit_cast(bf16x8, __builtin_shufflevector(lo, hi, 0, 1, 2, 3, 4, 5, 6, 7));
      }
    if (packer) { WVM(0); if (lane == 0) flag_set(a.sync + SY_W + gb * FS, tag); }
    bool have = false;
#pragma unroll 1
    for (int t0 = 0; t0 * 16 < nrow; t0 += 4) {
      const int t = t0 + rg;
      if (t0 == 4) { asm volatile("s_waitcnt vmcnt(5)\n\ts_barrier" ::: "memory"); flags_request(a.sync + SY_W, NPACK, lane); have = true; }
      if (t0 == 8) asm volatile("s_waitcnt vmcnt(8)\n\ts_barrier" ::: "memory");
      if (t0 == 12) asm volatile("s_waitcnt vmcnt(9)\n\ts_barrier" ::: "memory");
      if (t * 16 >= nrow) break;
      bf16x8 bs[2][4];
#pragma unroll
      for (int j = 0; j < 2; ++j)
#pragma unroll
        for (int ks = 0; ks < 4; ++ks) bs[j][ks] = *(const bf16x8*)(s_img + ((t + 2 * j) * 16 + r16) * 256 + 16 * ((4 * ks + q4) ^ r16));
#pragma unroll
      for (int j = 0; j < 2; ++j) {
        if (j && (t + 2) * 16 >= nrow) break;
        f32x4 acc[2] = {f32x4{0.f, 0.f, 0.f, 0.f}, f32x4{0.f, 0.f, 0.f, 0.f}};
#pragma unroll
        for (int ks = 0; ks < 4; ++ks)
#pragma unroll
          for (int m = 0; m < 2; ++m) acc[m] = __builtin_amdgcn_mfma_f32_16x16x32_bf16(aw[m][ks], bs[j][ks], acc[m], 0, 0, 0);
        u32x4 o;
#pragma unroll
        for (int i = 0; i < 4; ++i) o[i] = silu2(acc[i >> 1][2 * (i & 1)], acc[i >> 1][2 * (i & 1) + 1]);
        __builtin_amdgcn_raw_buffer_store_b128(o, rh, (u32)(row0 + (t + 2 * j) * 16 + r16) * 256u + ct * 64 + q4 * 16, 0, 16);
      }
    }
    TS(5);
    const u32x4* wp = a.wpack + w * WUNITS * 64;
    if (have) { if (nrow > 128) WVM(2); else WVM(0); have = flags_ok(tag); }
    if (have) { LDW2(wp); LDW4(154, wp); WVM(6); } else WVM(0);
    asm volatile("s_barrier" ::: "memory");
    if (tid == 0) flag_set(a.sync + SY_H + gb * FS, tag);
    TS(3);
    if (!have) { flags_wait(a.sync + SY_W, NPACK, tag, lane); LDW2(wp); LDW4(154, wp); }
  }
  touch(0, 2);
  TSOUT(0);
}

template <bool ONE>
__global__ __launch_bounds__(512, 2) __attribute__((amdgpu_num_vgpr(64))) void k_main(MainArgs) {
  KArgs* ap = (KArgs*)__builtin_amdgcn_kernarg_segment_ptr();
#define a (*ap)
  extern __shared__ __attribute__((aligned(16))) char smem[];
  u32 tag = 0;
  if (ONE) {
    if (threadIdx.x >= 256)
      asm volatile("s_getpc_b64 vcc\n\tv_min_u32 v132, painn_code_end-.-256, %0\n\tglobal_load_dword v132, v132, vcc"
                   :: "v"((threadIdx.x & 255) * 128) : "memory", "vcc");
    asm volatile("global_load_dword v131, %0, %1 sc1" :: "v"(0), "s"(a.sync),
                 "s"(a.sv), "s"(a.pos), "s"(a.src), "s"(a.dst), "s"(a.w1), "s"(a.w2), "s"(a.wr), "s"(a.N), "s"(a.epb), "s"(a.E), "s"(a.G), "s"(a.nbin), "s"(a.rpb) : "memory");
    first_phase(a, smem, tag);
    asm volatile("" : "+s"(ap));
    if ((int)blockIdx.x >= a.G) return;
  }
  const int tid0 = threadIdx.x;
  int w = __builtin_amdgcn_readfirstlane(tid0 >> 6);
  const int g = blockIdx.x;
  const u32 ldsb = (u32)(size_t)(__attribute__((address_space(3))) char*)smem;
  TS(0);
  int n0 = g * a.npg;
  const int nn = min(a.npg, a.N - n0);
  const int NS = (nn + 15) >> 4;
  u32* misc = (u32*)(smem + L_MISC);

  u32x4 rec;
  if (!ONE) {
    const u32* cp = a.cells + (size_t)g * (NCELL * CELLW) + tid0 * 4;
    asm volatile("global_load_dwordx4 %0, %1, off" : "=v"(rec) : "v"(cp) : "memory");
  }
  if (tid0 < 4) misc[16 + tid0] = 0;
  lds_barrier();

  bool first = true, cellbatch = true;
  int win = 0;
  int ovf_b = 0, ovf_pos = 0;
  u32 vmask = 0;

  for (;;) {
    int tid = tid0;
    asm volatile("" : "+s"(ap));
    u64 hp = (u64)a.h, svp = (u64)a.sv;
    u32 lds0 = ldsb;
    asm volatile("" : "+v"(tid), "+s"(w), "+s"(n0), "+s"(hp), "+s"(svp), "+s"(lds0));
    const int lane = tid & 63, r16 = lane & 15, q4 = lane >> 4;
    const u32x4* wp = a.wpack + w * WUNITS * 64;
    const u32x4 srdo = make_srd(a.out + (size_t)n0 * 512, nn * 1024);
    const u32x4 srdr = make_srd((first ? a.sv : a.out) + (size_t)n0 * 512, nn * 1024);
    u32x2 fq;
    asm volatile("global_load_dwordx2 %0, %1, %2" : "=v"(fq) : "v"((tid & 3) * 8), "s"(a.freqs) : "memory");
    const u32 frb = lds0 + L_FL + 2 * w * FLP;
    if (!ONE) { LDW2(wp); LDW4(154, wp); } else if (!first) load_weights(wp, lane);
    u32* list = (u32*)(smem + L_LIST);
    const int qq = (lane >> 2) & 3;
    u32 h0 = lds0 + (r16 >> 2) * HB + (r16 & 3) * 1024 + 16 * (q4 ^ (r16 & 3));
    u32 v0 = lds0 + q4 * HB + qq * 1024 + 256 + 32 * (w ^ ((qq & 1) | ((qq & 2) << 1))) + 8 * (lane & 3);
    u32 rhb = lds0 + L_RH + q4 * 48 + r16 * 4, dfb = lds0 + L_DF + ((w >> 1) * 4 + (lane & 3)) * 8;
    u32 sw = lds0 + L_S + (w & 1) * 1024 + (w >> 1) * 256 + lane * 4;
    u32 fw0 = lds0 + L_FL + r16 * FLP + w * 32 + q4 * 8;
    u32 skey = (u32)((w & 1) * 16 + (lane >> 2)) * 0x10001u;
    u32 ftr = lds0 + L_FL + (lane >> 2) * FLP + w * 32 + 8 * (lane & 3);
    u64 idn = (r16 >> 2) == q4 ? 0x3f80ull << (16 * (r16 & 3)) : 0ull;
    u64 gbase = (lane < 16 ? hp : svp) - 3072;
    u32 gsh = lane < 16 ? 8u : 10u;
    u32 lo[4];
#pragma unroll
    for (int k = 0; k < 4; ++k)
      lo[k] = 16 * (lane ^ (lane < 16 ? k : (((k & 1) << 1) | ((k & 2) << 2)))) + 3072 - 1024 * k;
    const u32 drow = lds0 + (4 * (w & 1) + (w >> 1)) * HB;
    int a_df = L_DF + (tid >> 1) * 8 + (tid & 1) * 2;
    int a_rh = L_RH + ((tid >> 5) * 96 + ((tid >> 2) & 1) * 48 + ((tid >> 3) & 3) * 12 + (tid & 3)) * 4;
    const int p0 = (7 - w) * 16 + (lane >> 2);
    int a_rbf = L_RBF + (p0 >> 5) * 1024 + ((lane & 3) * 16 + 4 * ((p0 >> 3) & 3) + (p0 & 3)) * 16 + ((p0 >> 2) & 1) * 8;
    asm volatile(SETUP_ASM :: "v"(h0), "v"(v0), "v"(lane * 16), "v"(rhb), "v"(dfb), "v"(sw), "v"(fw0), "v"(ftr), "v"(skey), "v"(gsh),
                 "v"(lo[0]), "v"(lo[1]), "v"(lo[2]), "v"(lo[3]), "v"(frb + lane * 16), "v"(gbase), "v"(idn));
    asm volatile("" : "+v"(a_df), "+v"(a_rh), "+v"(a_rbf));
    if (tid < CAPL) list[tid] = 0xff000000u | (u32)n0;
    if (ONE && cellbatch) {
      asm volatile("" : "+s"(tag));
      const u32x4 tg = cell_tags(tag);
      const u32 co = (min(tid >> 2, a.nbin - 1) * a.G + g) * (CELLW * 4) + (tid & 3) * 16;
      bool miss = true;
      rec = u32x4{co, co, co, co};
      if (w * 16 < a.nbin)
#pragma unroll 1
      for (int s = 0;; ++s) {
        if (miss) asm volatile("global_load_dwordx4 v[240:243], %0, %1 sc1" :: "v"(co), "s"(a.cells) : "memory");
        u64 r01, r23;
        asm volatile("s_waitcnt vmcnt(0)\n\tv_mov_b64 %0, v[240:241]\n\tv_mov_b64 %1, v[242:243]" : "=v"(r01), "=v"(r23) :: "memory");
        rec = u32x4{(u32)r01, (u32)(r01 >> 32), (u32)r23, (u32)(r23 >> 32)};
        rec ^= tg;
        miss = ((rec[0] | rec[1] | rec[2] | rec[3]) & TAG_MASK) != 0;
        if (__builtin_amdgcn_ballot_w64(miss) == 0 || s == SPIN_MAX) break;
        __builtin_amdgcn_s_sleep(6);
      }
      if (w == 6 && first) flags_request(a.sync + SY_H, 256 - a.nbin, lane);
      vmask = 0;
      miss = miss || (tid >> 2) >= a.nbin;
#pragma unroll
      for (int i = 0; i < 4; ++i) vmask |= (rec[i] < REC_MORE && !miss ? 1u : 0u) << i;
      if (rec[3] == REC_MORE && !miss) atomicOr(&misc[16 + (tid >> 7)], 1u << ((tid >> 2) & 31));
      TS(1);
    }
    if (!ONE && cellbatch) {
      asm volatile("s_waitcnt vmcnt(7)" : "+v"(rec) :: "memory");
      u32 c = __builtin_amdgcn_update_dpp(0, rec[0], 0x00, 0xf, 0xf, false);
      const int col = tid >> 2;
      if ((((col & 15) << 3) | (col >> 4)) >= NBIN) c = 0;
      if (c > CELLCAP && (tid & 3) == 0) atomicOr(&misc[16 + (col >> 5)], 1u << (col & 31));
      c = min(c, (u32)CELLCAP);
      vmask = ((((2u << c) - 2u) >> ((tid & 3) * 4)) & 15u);
      TS(1);
    }
    u64 loc = 0; u32 lr[4];
#pragma unroll
    for (int i = 0; i < 4; ++i) {
      const u32 sh = (rec[i] >> 25) & 56u;
      lr[i] = (u32)(loc >> sh) & 0xffu;
      loc += (u64)((vmask >> i) & 1u) << sh;
    }
    const u32 ilo = wave_incl_scan((u32)loc), ihi = wave_incl_scan((u32)(loc >> 32));
    if (lane == 63) *(u32x2*)(misc + 2 * w) = u32x2{ilo, ihi};
    const u64 ex = (((u64)ihi << 32) | ilo) - loc;
    const bool wlate = !ONE || first;
    if (wlate) LDW4(170, wp + 256);
    lds_barrier();
    const u32 cw = ((const unsigned char*)misc)[(lane & 7) * 8 + (lane >> 3)];
    const u32 inc = wave_incl_scan(cw);
    const int T = __builtin_amdgcn_readlane(inc, 63);
    const int Tn = min(T - win, WIN);
    const int exc = (int)(inc - cw) - win;
    int offv = min(max(exc, 0), Tn);
    int padv = 0;
    {
      const int prev = __builtin_amdgcn_ds_bpermute((lane - 8) * 4, offv);
      const bool bad = (lane & 7) == 0 && lane >= 16 && prev > 0 && ((prev - 1) >> 5) == (offv >> 5) && offv < Tn;
      if (__builtin_amdgcn_ballot_w64(bad) != 0) {
        int pad = 0, ps = 0;
#pragma unroll 1
        for (int sgi = 1; sgi < 8; ++sgi) {
          int st = __builtin_amdgcn_readlane(offv, 8 * sgi) + pad;
          if (sgi < 7 && ps > 0 && ((ps - 1) >> 5) == (st >> 5)) { const int np = (st + 31) & ~31; pad += np - st; st = np; }
          ps = st;
          if ((lane >> 3) == sgi) padv = pad;
        }
      }
    }
    offv += padv;
    const int nt = (__builtin_amdgcn_readlane(offv, 56) + 31) >> 5;
    if (wlate) LDW4(186, wp + 512);
    u32 base[4];
    const u32 excp = ((u32)padv << 16) | ((u32)(exc + 4096) & 0xffffu);
#pragma unroll
    for (int i = 0; i < 4; ++i) base[i] = __builtin_amdgcn_ds_bpermute((((rec[i] >> 25) & 56u) + w) * 4, excp);
#pragma unroll
    for (int i = 0; i < 4; ++i) {
      const u32 p = (base[i] & 0xffffu) - 4096u + ((u32)(ex >> ((rec[i] >> 25) & 56u)) & 0xffu) + lr[i];
      if ((vmask >> i & 1) && p < (u32)Tn) list[p + (base[i] >> 16)] = rec[i];
    }
    lds_barrier();
    TS(2);

    const u32 myrec = list[min(tid, CAPL - 1)];
    const bool live = myrec < 0x80000000u;
    const u32 mys = myrec & SRC_MASK, myd = live ? (u32)n0 + (myrec >> 24) : (u32)n0;
    u32x2 ps, pd;
    asm volatile("global_load_dwordx2 %0, %2, %4\n\tglobal_load_dwordx2 %1, %3, %4"
                 : "=&v"(ps), "=&v"(pd) : "v"((mys * 6u) & ~3u), "v"((myd * 6u) & ~3u), "s"(a.pos) : "memory");
    auto gather = [&](int t, int nb = 0) {
      const u32x4 l4 = *(const u32x4*)(list + t * 32 + 4 * (w + nb));
      asm volatile("s_mov_b32 m0, %4\n\ts_nop 0\n\tglobal_load_lds_dwordx4 %0, off sc1\n\tglobal_load_lds_dwordx4 %1, off offset:1024 sc1\n\t"
                   "global_load_lds_dwordx4 %2, off offset:2048 sc1\n\tglobal_load_lds_dwordx4 %3, off offset:3072 sc1"
                   :: "v"(gbase + ((l4[0] << gsh) + lo[0])), "v"(gbase + ((l4[1] << gsh) + lo[1])), "v"(gbase + ((l4[2] << gsh) + lo[2])),
                      "v"(gbase + ((l4[3] << gsh) + lo[3])), "s"(drow + t * TILEB + nb * 4 * HB) : "memory");
    };
    auto gather_v = [&](int t) { if (lane >= 16 && w != 6) { gather(t); if (w == 7) gather(t, -1); } };
    if (ONE) gather_v(0); else gather(0);
    u64 ends = 0xffull << 56;
    {
      const u32 fiv = (lane >> 3) <= NS ? (u32)(offv + 31) >> 5 : 0xffu;
#pragma unroll
      for (int sgi = 0; sgi < 7; ++sgi) ends |= (u64)(u32)__builtin_amdgcn_readlane(fiv, 8 * sgi + 8) << (8 * sgi);
    }
    asm volatile("" : "+s"(ends));
    if (ONE && w == 7) WVM(8); else if (ONE && w == 6) WVM(0); else WVM(4);
    asm volatile("" : "+v"(ps), "+v"(pd), "+v"(fq) :: "memory");
    TS(3);
    float rx = 0.f, ry = 0.f, rz = 0.f, d = 0.f, id = 0.f;
    const bool geo = w * 64 < nt * 32;
    if (geo) {
      d = rb(__builtin_amdgcn_sqrtf(rb(rb(edge_sq(ps, mys, pd, myd, rx, ry, rz)) + 1e-8f)));
      id = live ? __builtin_amdgcn_rcpf(d) : 0.f;
      *(f32x2*)(smem + L_GD + tid * 8) = f32x2{d, id};
    }
    if (ONE && w == 6 && first && !flags_ok(tag)) flags_wait(a.sync + SY_H, 256 - a.nbin, tag, lane);
    lds_barrier();
    TS(5);
    if (ONE && first && g == 0 && tid == 0) asm volatile("global_store_dword %0, %1, %2 sc1" :: "v"(tid), "v"(tag), "s"(a.sync) : "memory");
    if (ONE && lane < 16) gather(0);
    gather(1);
    if (geo && tid < CAPL) {
      const float ca = rb(rb(3.141592653589793f * d) * 0.2f);
      float fc = 0.5f * rb(rb(__builtin_amdgcn_cosf(red2pi(ca))) + 1.0f);
      fc = (live && d < 5.0f) ? fc : 0.f;
      u16* df = (u16*)(smem + a_df);
      df[0] = (u16)((myrec >> 24) & 31u);
      df[2] = (u16)(__float_as_uint(fc) >> 16);
      float* rh = (float*)(smem + a_rh);
      const u32 rxy = pk2(rx * id, ry * id);
      rh[0] = bflo(rxy); rh[4] = bfhi(rxy); rh[8] = rb(rz * id);
    }
#pragma unroll 1
    for (int p = (7 - w) * 16; p < nt * 32; p += 128) {
      const f32x2 gd = *(const f32x2*)(smem + L_GD + p * 8 + (lane >> 2) * 8);
      const u32 x01 = pk2(gd[0] * bflo(fq[0]), gd[0] * bfhi(fq[0])), x23 = pk2(gd[0] * bflo(fq[1]), gd[0] * bfhi(fq[1]));
      const u32 s01 = pk2(__builtin_amdgcn_sinf(red2pi(bflo(x01))), __builtin_amdgcn_sinf(red2pi(bfhi(x01))));
      const u32 s23 = pk2(__builtin_amdgcn_sinf(red2pi(bflo(x23))), __builtin_amdgcn_sinf(red2pi(bfhi(x23))));
      *(u32x2*)(smem + a_rbf) = u32x2{pk2(bflo(s01) * gd[1], bfhi(s01) * gd[1]), pk2(bflo(s23) * gd[1], bfhi(s23) * gd[1])};
      a_rbf += 4096;
    }
    TS(4);

    int ns, cur;
    {
      asm volatile(LOOP_ASM
                   : "=&s"(ns), "=&s"(cur)
                   : "s"(nt), "s"(ends), "s"(srdr), "s"(srdo), "s"(drow), "s"(w * 2048), "s"(lds0 + L_LIST + 16 * w), "s"(lds0), "s"(__builtin_amdgcn_readfirstlane(frb))
                   : "memory", "scc", "vcc", LOOP_CLOB);
    }
    if (ONE) asm volatile("painn_code_end:");
    TS(6);
    int tz = tid0;
    asm volatile("" : "+v"(tz), "+s"(ap));
    vmask = tz; rec = u32x4{vmask, vmask, vmask, vmask};
    lds_barrier();
    {
      const auto rs_out = __builtin_amdgcn_make_buffer_rsrc((void*)(a.out + (size_t)n0 * 512), 0, nn * 1024, 0x00020000);
      const int l16 = (tz & 63) * 16;
#pragma unroll 1
      for (; ns < cur; ++ns) {
        const char* fb = smem + L_FL + (ns & 1) * FLB + 2 * w * FLP + l16;
        const u32x4 x0 = *(const u32x4*)fb, x1 = *(const u32x4*)(fb + FLP);
        __builtin_amdgcn_raw_buffer_store_b128(x0, rs_out, l16, ns * 16384 + w * 2048, 2);
        __builtin_amdgcn_raw_buffer_store_b128(x1, rs_out, l16 + 1024, ns * 16384 + w * 2048, 2);
      }
    }
    TS(7);

    const u32x4 fl = *(const u32x4*)(misc + 16);
    const bool any = __builtin_amdgcn_readfirstlane(fl[0] | fl[1] | fl[2] | fl[3]) != 0 && ovf_b < NCELL;
    first = false;
    win += WIN;
    if (win >= T && !any) break;
    WVM(0); lds_barrier();
    if (win < T) {
      if (ONE) continue;
      rec = *(const u32x4*)(a.cells + (size_t)g * (NCELL * CELLW) + tz * 4);
      asm volatile("" : "+v"(rec));
      vmask = 0;
      continue;
    }
    cellbatch = false;
    u32* stage = (u32*)smem;
    if (tz == 0) misc[20] = 0;
    lds_barrier();
    int sc = 0;
    while (ovf_b < NCELL && sc <= WIN - 128) {
      if (!(__builtin_amdgcn_readfirstlane(misc[16 + (ovf_b >> 5)]) >> (ovf_b & 31) & 1)) { ++ovf_b; continue; }
      const int b = ONE ? ovf_b : ((ovf_b & 15) << 3) | (ovf_b >> 4);
      const int n = __builtin_amdgcn_readfirstlane(__hip_atomic_load(a.ovfcnt + b, __ATOMIC_RELAXED, __HIP_MEMORY_SCOPE_AGENT));
      const int chunk = WIN - sc;
      if (tz < chunk && ovf_pos + tz < n) {
        const u64 e = __hip_atomic_load(a.ovf + (size_t)b * a.epb + ovf_pos + tz, __ATOMIC_RELAXED, __HIP_MEMORY_SCOPE_AGENT);
        if ((u32)(e >> 32) == (u32)g) stage[atomicAdd(&misc[20], 1u)] = (u32)e;
      }
      ovf_pos += chunk;
      if (ovf_pos >= n) { ovf_pos = 0; ++ovf_b; }
      lds_barrier();
      sc = __builtin_amdgcn_readfirstlane(misc[20]);
      lds_barrier();
    }
    vmask = tz < sc ? 1u : 0u;
    rec = u32x4{tz < sc ? stage[tz] : 0u, 0u, 0u, 0u};
    win = 0;
    WVM(0); lds_barrier();
  }
}

#undef a

extern "C" void kernel_launch(void* const* d_in, const int* in_sizes, int n_in, void* d_out, int out_size, void* d_ws,
                              size_t ws_size, hipStream_t stream) {
  const int N = in_sizes[1] / 3, E = in_sizes[2];
  int npg = (N + 255) / 256; if (npg > NPG_MAX) npg = NPG_MAX; if (npg < 16) npg = 16;
  const int G = (N + npg - 1) / npg;
  int nbin = (E + EPB1 - 1) / EPB1; if (nbin < 64) nbin = 64;
  const int rpb = nbin <= NBIN ? ((N + 255 - nbin) / (256 - nbin) + 15) & ~15 : 0;
  int dev = 0, ncu = 0;
  (void)hipGetDevice(&dev);
  (void)hipDeviceGetAttribute(&ncu, hipDeviceAttributeMultiprocessorCount, dev);
  const bool one = !FORCE2 && ncu >= 256 && G <= 256 && N <= 32768 && nbin <= NBIN && rpb <= 224 && ((N * 6 + 15) & ~15) + 1028 <= L_MAIN_TOTAL;
  if (!one) nbin = NBIN;
  const int epb = (((E + nbin - 1) / nbin) + 3) & ~3;
  char* ws = (char*)d_ws;
  size_t o = 0;
  auto take = [&](size_t bytes) { char* p = ws + o; o += (bytes + 255) & ~(size_t)255; return p; };
  u32* cells = (u32*)take((size_t)G * NCELL * CELLW * 4);
  u32x4* wpack = (u32x4*)take(8 * WUNITS * 64 * 16);
  u32* ovfcnt = (u32*)take(NCELL * 4);
  u16* h = (u16*)take((size_t)N * 256);
  u64* ovf = (u64*)take((size_t)NBIN * epb * 8);
  u32* sync = (u32*)take((SY_H + 192 * FS) * 4);

  PrepArgs p;
  p.sv = (const u16*)d_in[0]; p.pos = (const u16*)d_in[1]; p.src = (const int*)d_in[2]; p.dst = (const int*)d_in[3];
  p.w1 = (const u16*)d_in[5]; p.w2 = (const u16*)d_in[6]; p.wr = (const u16*)d_in[7];
  p.cells = cells; p.ovf = ovf; p.ovfcnt = ovfcnt; p.h = h; p.wpack = wpack; p.ws = ws;
  p.N = N; p.E = E; p.epb = epb; p.G = G; p.npg = npg; p.npg_magic = (u32)((0x100000000ull + npg - 1) / npg);
  MainArgs m;
  m.sv = p.sv; m.pos = p.pos; m.freqs = (const u16*)d_in[4]; m.cells = cells; m.ovf = ovf; m.ovfcnt = ovfcnt;
  m.h = h; m.wpack = wpack; m.out = (u16*)d_out; m.ws = ws; m.src = p.src; m.dst = p.dst; m.w1 = p.w1; m.w2 = p.w2; m.wr = p.wr;
  m.sync = sync; m.N = N; m.epb = epb; m.npg = npg; m.E = E; m.G = G; m.npg_magic = p.npg_magic; m.nbin = nbin; m.rpb = rpb;
  if (one) {
    (void)hipFuncSetAttribute((const void*)k_main<true>, hipFuncAttributeMaxDynamicSharedMemorySize, L_MAIN_TOTAL);
    k_main<true><<<256, 512, L_MAIN_TOTAL, stream>>>(m);
    return;
  }
  const int ngemm = (((G + 1) / 2) + 7) & ~7;
  const int lds_gemm = 32768 + 2 * NPG_MAX * 256;
  const int lds_bin = ((N * 6 + 15) & ~15) + (G + 1) * 4;
  if (lds_bin <= 163840) {
    (void)hipFuncSetAttribute((const void*)k_prep<true>, hipFuncAttributeMaxDynamicSharedMemorySize, 163840);
    k_prep<true><<<NBIN + ngemm, 1024, lds_bin > lds_gemm ? lds_bin : lds_gemm, stream>>>(p);
  } else {
    (void)hipFuncSetAttribute((const void*)k_prep<false>, hipFuncAttributeMaxDynamicSharedMemorySize, 163840);
    k_prep<false><<<NBIN + ngemm, 1024, lds_gemm > (G + 1) * 4 ? lds_gemm : (G + 1) * 4, stream>>>(p);
  }
  (void)hipFuncSetAttribute((const void*)k_main<false>, hipFuncAttributeMaxDynamicSharedMemorySize, L_MAIN_TOTAL);
  k_main<false><<<G, 512, L_MAIN_TOTAL, stream>>>(m);
}
```
